```python
import torch

def _softmax(x, axis=-1):
    return torch.softmax(x, dim=axis)
def _gelu(x):
    return torch.nn.functional.gelu(x, approximate="tanh")
def _silu(x):
    return torch.nn.functional.silu(x)
def _sigmoid(x):
    return torch.sigmoid(x)
def _layernorm(x, axis=-1, eps=1e-5):
    m = x.mean(dim=axis, keepdim=True)
    v = x.var(dim=axis, keepdim=True, unbiased=False)
    return (x - m) / torch.sqrt(v + eps)
def _scatter_add(tgt, idx, src, axis=0):
    return tgt.clone().index_add_(axis, idx, src)
def _segment_sum(src, seg, n):
    out = torch.zeros((n,) + src.shape[1:], dtype=src.dtype)
    return out.index_add_(0, seg, src)
def _pad(x, pw):
    flat = tuple(v for pair in reversed(pw) for v in pair)
    return torch.nn.functional.pad(x, flat)

kernel_name = 'concat_heads__grouped_kv_mqa__spectral_fnet__prompt'

def setup_inputs():

    _w = (lambda a: (lambda t: t.to(torch.bfloat16) if t.is_floating_point() else t)(torch.as_tensor(a)))
    def _A_setup():
        B, S, D, H = 1, 2048, 576, 32
        Dh = D // H
        return {
            "x": _w((lambda *s: torch.randn(*s, dtype=torch.bfloat16) * 0.05)(B, S, D)),
            "wq": _w((lambda *s: torch.randn(*s, dtype=torch.bfloat16) * 3.2)(D, H * Dh)),
            "wk": _w((lambda *s: torch.randn(*s, dtype=torch.bfloat16) * 3.2)(D, Dh)),
            "wv": _w((lambda *s: torch.randn(*s, dtype=torch.bfloat16) * 0.4)(D, Dh)),
            "wo": _w((lambda *s: torch.randn(*s, dtype=torch.bfloat16) * 0.4)(H * Dh, D)),
        }
    _A_in = _A_setup()

    def _B_setup():
        B, S, D = 1, 2048, 576
        ns = torch.arange(S)[:, None] * torch.arange(S)[None, :]
        nd = torch.arange(D)[:, None] * torch.arange(D)[None, :]
        Ws_re = torch.cos(-2.0 * torch.pi * ns / S).to(float)
        Ws_im = torch.sin(-2.0 * torch.pi * ns / S).to(float)
        Wd_re = torch.cos(-2.0 * torch.pi * nd / D).to(float)
        Wd_im = torch.sin(-2.0 * torch.pi * nd / D).to(float)
        return {
            "x": _w((lambda *s: torch.randn(*s, dtype=torch.bfloat16) * 0.05)(B, S, D)),
            "dft_seq_re": _w(Ws_re),
            "dft_seq_im": _w(Ws_im),
            "dft_hid_re": _w(Wd_re),
            "dft_hid_im": _w(Wd_im),
        }
    _B_in = _B_setup()

    return {
        "A_x": _A_in["x"],
        "A_wq": _A_in["wq"],
        "A_wk": _A_in["wk"],
        "A_wv": _A_in["wv"],
        "A_wo": _A_in["wo"],
        "B_x": _B_in["x"],
        "B_dft_seq_re": _B_in["dft_seq_re"],
        "B_dft_seq_im": _B_in["dft_seq_im"],
        "B_dft_hid_re": _B_in["dft_hid_re"],
        "B_dft_hid_im": _B_in["dft_hid_im"],
    }

def reference(A_x, A_wq, A_wk, A_wv, A_wo, B_x, B_dft_seq_re, B_dft_seq_im, B_dft_hid_re, B_dft_hid_im):
    def _A_ref(x, wq, wk, wv, wo):
        B, S, D = x.shape
        H = 32
        Dh = D // H
        q = (lambda x, *d: x.permute(*d))((x @ wq).reshape(B, S, H, Dh), 0, 2, 1, 3)
        k = x @ wk
        v = x @ wv
        scores = torch.einsum("bhqd,bkd->bhqk", q, k) * (Dh ** -0.5)
        p = _softmax(scores, axis=-1)
        o = torch.einsum("bhqk,bkd->bhqd", p, v)
        return (lambda x, *d: x.permute(*d))(o, 0, 2, 1, 3).reshape(B, S, H * Dh) @ wo

    def _B_ref(x, dft_seq_re, dft_seq_im, dft_hid_re, dft_hid_im):
        a = torch.einsum("bsd,ed->bse", x, dft_hid_re)
        b = torch.einsum("bsd,ed->bse", x, dft_hid_im)
        y = torch.einsum("ts,bse->bte", dft_seq_re, a) - torch.einsum("ts,bse->bte", dft_seq_im, b)
        return y

    a = _A_ref(A_x, A_wq, A_wk, A_wv, A_wo)
    b = _B_ref(B_x, B_dft_seq_re, B_dft_seq_im, B_dft_hid_re, B_dft_hid_im)
    return torch.concatenate([a, b], axis=-1)
```

```cpp
#include <hip/hip_runtime.h>
#include <hip/hip_bf16.h>
#include <stdint.h>

typedef __attribute__((ext_vector_type(8))) __bf16 bf16x8;
typedef __attribute__((ext_vector_type(16))) __bf16 bf16x16;
typedef __attribute__((ext_vector_type(2))) __bf16 bf16x2;
typedef __attribute__((ext_vector_type(4))) short s16x4;
typedef __attribute__((ext_vector_type(16))) float f32x16;
typedef __attribute__((ext_vector_type(8))) float f32x8;
typedef __attribute__((ext_vector_type(4))) float f32x4;
typedef __attribute__((ext_vector_type(2))) float f32x2;
typedef __attribute__((ext_vector_type(4))) unsigned u32x4;
typedef __attribute__((ext_vector_type(2))) unsigned u32x2;
typedef __attribute__((address_space(3))) s16x4* lds_s16x4_p;
typedef __attribute__((address_space(3))) unsigned* lds_u32_p;
typedef __attribute__((address_space(1))) unsigned long long gu64;
typedef unsigned long long u64;
typedef __amdgpu_buffer_rsrc_t rsrc_t;

constexpr unsigned C_E = 0;
constexpr unsigned C_FAB = 4096;
constexpr unsigned C_FP = C_FAB + 32768;
constexpr unsigned C_FO = C_FP + 32768;
constexpr unsigned C_FWO = C_FO + 32768;
constexpr unsigned C_FKV = C_FWO + 8192;
constexpr unsigned WS_KV = C_FKV + 8192;
constexpr unsigned WS_ABB = WS_KV + 163840;
constexpr unsigned WS_ABS = WS_ABB + 4194304;
constexpr unsigned WS_WOT = WS_ABS + 524288;
constexpr unsigned WS_WOS = WS_WOT + 589824;
constexpr unsigned WS_O = WS_WOT + 663552;
constexpr unsigned WS_P = WS_O + 2359296;
constexpr unsigned WS_END = WS_P + 8388608;
#define SB() __builtin_amdgcn_sched_barrier(0)

#define KXV ((a >> 3) & 3)
#define RFL(x) __builtin_amdgcn_readfirstlane(x)
#define WAITV(n) asm volatile("s_waitcnt vmcnt(" #n ")" ::: "memory")
#define BARRIER() do { asm volatile("" ::: "memory"); __builtin_amdgcn_s_waitcnt(0xC07F); __builtin_amdgcn_s_barrier(); asm volatile("" ::: "memory"); } while (0)

__device__ __forceinline__ unsigned pk2(float a, float b) {
  f32x2 v = {a, b};
  return __builtin_bit_cast(unsigned, __builtin_convertvector(v, bf16x2));
}
__device__ __forceinline__ unsigned pk2s(float a, float b) { unsigned v = pk2(a, b); return v == ~0u ? 0xfffeffffu : v; }
__device__ __forceinline__ u32x4 pack_half(const f32x16& c, int u, float sc) {
  unsigned a0 = pk2(c[8 * u + 0] * sc, c[8 * u + 1] * sc), a1 = pk2(c[8 * u + 2] * sc, c[8 * u + 3] * sc);
  unsigned b0 = pk2(c[8 * u + 4] * sc, c[8 * u + 5] * sc), b1 = pk2(c[8 * u + 6] * sc, c[8 * u + 7] * sc);
  auto r0 = __builtin_amdgcn_permlane32_swap(a0, b0, false, false);
  auto r1 = __builtin_amdgcn_permlane32_swap(a1, b1, false, false);
  return u32x4{r0[0], r1[0], r0[1], r1[1]};
}
__device__ __forceinline__ f32x16 mfma32(u32x4 a, u32x4 b, f32x16 c) {
  return __builtin_amdgcn_mfma_f32_32x32x16_bf16(__builtin_bit_cast(bf16x8, a), __builtin_bit_cast(bf16x8, b), c, 0, 0, 0);
}
__device__ __forceinline__ f32x4 mfma16(u32x4 a, u32x4 b, f32x4 c) {
  return __builtin_amdgcn_mfma_f32_16x16x32_bf16(__builtin_bit_cast(bf16x8, a), __builtin_bit_cast(bf16x8, b), c, 0, 0, 0);
}
__device__ __forceinline__ f32x16 mfma32k8(u32x2 a, u32x2 b, f32x16 c) {
  return __builtin_amdgcn_mfma_f32_32x32x8bf16_1k(__builtin_bit_cast(s16x4, a), __builtin_bit_cast(s16x4, b), c, 0, 0, 0);
}
__device__ __forceinline__ u32x4 tr_frag(const char* p, int second) {
  u32x2 a = __builtin_bit_cast(u32x2, __builtin_amdgcn_ds_read_tr16_b64_v4i16((lds_s16x4_p)p));
  u32x2 b = __builtin_bit_cast(u32x2, __builtin_amdgcn_ds_read_tr16_b64_v4i16((lds_s16x4_p)(p + second)));
  return u32x4{a[0], a[1], b[0], b[1]};
}
__device__ __forceinline__ u32x4 mk_srd(const void* p, unsigned bytes) {
  return u32x4{(unsigned)(size_t)p, (unsigned)((size_t)p >> 32), bytes, 0x20000u};
}
template <bool SC1>
__device__ __forceinline__ void glds(u32x4 srd, unsigned voff, unsigned soff, unsigned m0v) {
  m0v = RFL(m0v);
  if (SC1) asm volatile("s_mov_b32 m0, %3\n s_nop 0\n buffer_load_dwordx4 %0, %1, %2 offen sc1 lds" :: "v"(voff), "s"(srd), "s"(soff), "s"(m0v) : "memory");
  else asm volatile("s_mov_b32 m0, %3\n s_nop 0\n buffer_load_dwordx4 %0, %1, %2 offen lds" :: "v"(voff), "s"(srd), "s"(soff), "s"(m0v) : "memory");
}
__device__ __forceinline__ void st16(rsrc_t r, unsigned off, u32x4 v) { __builtin_amdgcn_raw_buffer_store_b128(v, r, off, 0, 16); }
__device__ __forceinline__ void st8(rsrc_t r, unsigned off, u32x2 v) { __builtin_amdgcn_raw_buffer_store_b64(v, r, off, 0, 16); }
__device__ __forceinline__ void st4(rsrc_t r, unsigned off, unsigned v) { __builtin_amdgcn_raw_buffer_store_b32(v, r, off, 0, 16); }
__device__ __forceinline__ u64 mk_tag(u64 e0) {
  return (((u64)(unsigned)RFL((unsigned)(e0 >> 32)) << 32) | (u64)(unsigned)RFL((unsigned)e0)) + 0x9E3779B97F4A7C15ull;
}
__device__ __forceinline__ void set_flag(rsrc_t r, unsigned off, u64 tag) {
  st16(r, off, u32x4{(unsigned)tag, (unsigned)(tag >> 32), (unsigned)tag, (unsigned)(tag >> 32)});
}
__device__ __forceinline__ bool flags_ok(u32x4 v, u64 tag) {
  const unsigned lo = (unsigned)tag, hi = (unsigned)(tag >> 32);
  return v[0] == lo && v[1] == hi && v[2] == lo && v[3] == hi;
}
__device__ __forceinline__ void wait_flags(rsrc_t r, unsigned off, u64 tag) {
#pragma unroll 1
  for (unsigned spins = 0; spins < (1u << 18); ++spins) {
    asm volatile("" ::: "memory");
    u32x4 v = __builtin_amdgcn_raw_buffer_load_b128(r, off, 0, 16);
    if (__builtin_amdgcn_ballot_w64(!flags_ok(v, tag)) == 0) break;
    __builtin_amdgcn_s_sleep(1);
  }
  asm volatile("" ::: "memory");
}

__device__ __forceinline__ float max16(const f32x16& s) {
  float a = fmaxf(fmaxf(fmaxf(s[0], s[1]), fmaxf(s[2], s[3])), fmaxf(fmaxf(s[4], s[5]), fmaxf(s[6], s[7])));
  float b = fmaxf(fmaxf(fmaxf(s[8], s[9]), fmaxf(s[10], s[11])), fmaxf(fmaxf(s[12], s[13]), fmaxf(s[14], s[15])));
  return fmaxf(a, b);
}
__device__ __forceinline__ float xhalf(float v, int h) {
  unsigned u = __builtin_bit_cast(unsigned, v);
  auto r = __builtin_amdgcn_permlane32_swap(u, u, false, false);
  return __builtin_bit_cast(float, h ? r[0] : r[1]);
}

__device__ __forceinline__ unsigned kv_tile(int t) { return WS_KV + (t >> 3) * 20480 + (t & 7) * 2240; }
__device__ __forceinline__ unsigned kv_k2(int t) { return WS_KV + (t >> 3) * 20480 + 17920 + ((t >> 2) & 1) * 528 + (t & 3) * 4; }
__device__ __forceinline__ unsigned v_off(int r, int h) { return 1024 + (r < 18 ? h * 288 + r * 16 : (r == 18 ? 576 : 592)); }
__device__ __forceinline__ f32x16 att_exact(rsrc_t rw, u32x4 q1, unsigned q2x, int lane) {
  const int r = lane & 31, h = lane >> 5;
  const u32x2 q2 = {q2x, 0u};
  float m = -__builtin_inff();
  const f32x16 z = {0};
  f32x16 o = {0};
#pragma unroll 1
  for (int it = 0; it < 128; ++it) {
    const int t = it & 63;
    u32x4 k1 = __builtin_amdgcn_raw_buffer_load_b128(rw, kv_tile(t) + lane * 16, 0, 16);
    u32x2 k2 = {__builtin_amdgcn_raw_buffer_load_b32(rw, kv_k2(t) + r * 16, 0, 16), 0u};
    f32x16 s = mfma32k8(k2, q2, mfma32(k1, q1, z));
    if (it < 64) {
      m = fmaxf(m, max16(s));
      if (it == 63) m = fmaxf(m, xhalf(m, h));
    } else {
      u32x4 v0 = __builtin_amdgcn_raw_buffer_load_b128(rw, kv_tile(t) + v_off(r, h), 0, 16);
      u32x4 v1 = __builtin_amdgcn_raw_buffer_load_b128(rw, kv_tile(t) + v_off(r, h) + 608, 0, 16);
      f32x8 p0, p1;
#pragma unroll
      for (int i = 0; i < 8; ++i) { p0[i] = __builtin_amdgcn_exp2f(s[i] - m); p1[i] = __builtin_amdgcn_exp2f(s[8 + i] - m); }
      o = __builtin_amdgcn_smfmac_f32_32x32x32_bf16(__builtin_bit_cast(bf16x8, v0), __builtin_bit_cast(bf16x16, p0), o, (int)0xdddddddd, 0, 0);
      o = __builtin_amdgcn_smfmac_f32_32x32x32_bf16(__builtin_bit_cast(bf16x8, v1), __builtin_bit_cast(bf16x16, p1), o, (int)0xdddddddd, 0, 0);
    }
  }
  return o;
}
__device__ __forceinline__ bool att_bad(const f32x16& o, int h) {
  unsigned lo = 0xffffffffu, hi = 0u;
#pragma unroll
  for (int i = 0; i < 10; ++i) {
    const float f = (i >= 8 && h) ? o[0] : o[i];
    const unsigned a = __float_as_uint(f) & 0x7fffffffu;
    lo = a < lo ? a : lo; hi = a > hi ? a : hi;
  }
  const float lf = o[10];
  const unsigned l = h ? 0x3f800000u : __float_as_uint(lf);
  return lo < (5u << 23) || hi >= (247u << 23) || l < ((125u - 96) << 23) || l >= (247u << 23);
}

__device__ __forceinline__ float bf_lo(unsigned u) { return __builtin_bit_cast(float, u << 16); }
__device__ __forceinline__ float bf_hi(unsigned u) { return __builtin_bit_cast(float, u & 0xffff0000u); }
struct QFr { u32x4 w[4], x0[4], x1[4]; };
struct AFr { u32x4 w[4], x0[4], x1[4], sw[2], sx[2]; };

__global__ void __launch_bounds__(256, 2)
fused_kernel(const char* __restrict__ Ax, const char* __restrict__ wq, const char* __restrict__ wk, const char* __restrict__ wv,
             const char* __restrict__ wo, const char* __restrict__ Bx, const char* __restrict__ Wsre, const char* __restrict__ Wsim,
             const char* __restrict__ Wdre, const char* __restrict__ Wdim, char* __restrict__ out, char* ws) {
  __shared__ __attribute__((aligned(16))) char shm[81920];
  const unsigned lb = (unsigned)(size_t)(lds_u32_p)shm;
  const int a = blockIdx.x & 255, R = (a & 7) * 4 + (a >> 6), g8 = (a >> 3) & 7;
  const int lane = threadIdx.x & 63, w = RFL(threadIdx.x >> 6), r = lane & 31, h = lane >> 5;
  const rsrc_t rw = __builtin_amdgcn_make_buffer_rsrc(ws, 0, WS_END, 0x20000);
  const rsrc_t ro = __builtin_amdgcn_make_buffer_rsrc(out, 0, 4718592, 0x20000);
  const unsigned vl = lane * 16;
  const int rot = (g8 + ((0x7520 >> (4 * (R & 3))) & 15)) % 9;
  u64 e0 = __hip_atomic_load((gu64*)(ws + C_E), __ATOMIC_RELAXED, __HIP_MEMORY_SCOPE_AGENT);
#define E0USE() asm volatile("" : "+v"(e0))
  asm volatile("s_nop 4" ::: "memory");
  if (blockIdx.x < 256) {
    __builtin_amdgcn_s_setprio(2);
    const unsigned obase = WS_O + R * 73728 + g8 * 9216;
    const u32x4 srdX = mk_srd(Ax + R * 73728, 73728u), srdQ = mk_srd(wq, 663552u);
    const int r0 = 16 * w + (lane >> 3), r1 = r0 + 8;
    const unsigned vx0 = r0 * 1152 + (((lane & 7) ^ ((r0 >> 1) & 7)) << 4), vx1 = r1 * 1152 + (((lane & 7) ^ ((r1 >> 1) & 7)) << 4);
    const int i0 = 128 * w + lane, i1 = i0 + 64, i2 = 512 + lane;
    const unsigned vq0 = (i0 / 9) * 1152 + g8 * 144 + (i0 % 9) * 16, vq1 = (i1 / 9) * 1152 + g8 * 144 + (i1 % 9) * 16,
                   vq2 = (i2 / 9) * 1152 + g8 * 144 + (i2 % 9) * 16;
    auto piece = [&](int k, int i) {
      unsigned d = lb + 12288 + ((k + 3) & 3) * 17408 + w * 2048;
      k += rot; k = k >= 9 ? k - 9 : k;
      if (i == 0) glds<false>(srdX, vx0, k * 128, d);
      if (i == 1) glds<false>(srdX, vx1, k * 128, d + 1024);
      if (i == 2) glds<false>(srdQ, vq0, k * 73728, d + 8192);
      if (i == 3) glds<false>(srdQ, vq1, k * 73728, d + 8192 + 1024);
      if (i == 4 && w == 0) glds<false>(srdQ, vq2, k * 73728, d + 16384);
    };
#pragma unroll 1
    for (int k = 0; k < 4; ++k) { piece(k, 4); piece(k, 0); piece(k, 1); piece(k, 2); piece(k, 3); }
    const int xoff = r * 128 + ((h ^ ((r >> 1) & 7)) << 4);
    int pc = 4 * ((lane >> 4) & 1) + (lane & 3); pc = pc > 4 ? 4 : pc;
    const int woff = 8192 + (8 * h + ((lane & 15) >> 2)) * 144 + ((w * 36) & ~7) + pc * 8;
    f32x16 c0 = {0}, c1 = {0};
    const u32x4 srdK = mk_srd(ws, WS_END);
    const int rh = g8 >> 2, ct = g8 & 3;
    u32x4 wf[36];
    const unsigned wb = WS_WOT + (ct * 4 + w) * 36864;
    auto kvp = [&](int n) { unsigned o = (n >= 5 ? 15360 : 0) + w * 5120 + n * 1024; glds<true>(srdK, vl, WS_KV + o, lb + o); };
    auto rdq = [&](QFr& f, int k, int ks) {
      const char* sb = shm + 12288 + ((k + 3) & 3) * 17408;
      f.w[ks] = tr_frag(sb + woff + ks * 2304, 576);
      f.x0[ks] = *(const u32x4*)(sb + (xoff ^ (ks << 5)));
      f.x1[ks] = *(const u32x4*)(sb + (xoff ^ (ks << 5)) + 4096);
    };
    auto itq = [&](int k, QFr& cur, QFr& nxt, bool ld) {
      BARRIER();
#pragma unroll
      for (int ks = 0; ks < 4; ++ks) {
        SB(); c0 = mfma32(cur.w[ks], cur.x0[ks], c0);
        SB(); rdq(nxt, k + 1, ks);
        SB(); c1 = mfma32(cur.w[ks], cur.x1[ks], c1);
        SB(); if (ld) { if (ks == 0) piece(k + 4, 4); piece(k + 4, ks); }
        if (k == 7) { kvp(ks * 5 / 2); kvp(ks * 5 / 2 + 1); if (ks & 1) kvp(ks * 5 / 2 + 2); }
      }
      SB();
    };
    u64 tag;
    {
      QFr fa, fb;
      u32x4 fl;
      const unsigned flo = (w ? C_FWO : C_FKV) + (a & 7) * 1024 + vl;
      WAITV(12);
      BARRIER();
#pragma unroll
      for (int ks = 0; ks < 4; ++ks) rdq(fa, 0, ks);
#pragma unroll
      for (int k = 0; k < 8; k += 2) {
        if (k < 6) WAITV(8); else WAITV(4);
        if (k == 0) __builtin_amdgcn_s_setprio(3);
        if (k == 6 && w < 2) fl = __builtin_amdgcn_raw_buffer_load_b128(rw, flo, 0, 16);
        itq(k, fa, fb, k < 5);
        if (k < 6) WAITV(8);
        else
        {
          WAITV(0);
          E0USE();
          tag = mk_tag(e0);
          if (w < 2 && __builtin_amdgcn_ballot_w64(!flags_ok(fl, tag))) wait_flags(rw, flo, tag);
        }
        itq(k + 1, fb, fa, k < 4);
      }
#pragma unroll
      for (int ks = 0; ks < 4; ++ks) {
        SB(); c0 = mfma32(fa.w[ks], fa.x0[ks], c0); c1 = mfma32(fa.w[ks], fa.x1[ks], c1);
        SB();
#pragma unroll
        for (int i = ks * 12 / 4; i < (ks + 1) * 12 / 4; ++i) wf[i] = __builtin_amdgcn_raw_buffer_load_b128(rw, vl + (i & 3) * 1024, wb + (i >> 2) * 4096, 16);
      }
      SB();
    }
    const float qs = 0.34004648f;
    u32x4 q1[2]; unsigned q2x[2];
#pragma unroll
    for (int g = 0; g < 2; ++g) {
      const f32x16& c = g ? c1 : c0;
      unsigned p[6];
#pragma unroll
      for (int i = 0; i < 6; ++i) p[i] = pk2(c[2 * i] * qs, c[2 * i + 1] * qs);
      if (w & 1) {
        auto s0 = __builtin_amdgcn_permlane32_swap(p[0], p[2], false, false);
        auto s1 = __builtin_amdgcn_permlane32_swap(p[2], p[4], false, false);
        q1[g] = u32x4{p[1], h ? s0[0] : s0[1], p[3], h ? s1[0] : s1[1]};
        q2x[g] = h ? 0u : p[5];
      } else {
        q1[g] = u32x4{p[0], p[1], p[2], p[3]};
        q2x[g] = h ? 0u : p[4];
      }
    }
    unsigned ak1 = lb + vl, ak2 = lb + 17920 + (h ? 512 : r * 16), av = lb + v_off(r, h);
    asm volatile("s_waitcnt vmcnt(%0)" :: "n"(12) : "memory");
    BARRIER();
    u32x4 q2a, q2b;
    {
      u32x4 k1 = *(const u32x4*)(shm + (ak1 - lb));
      u32x2 k2 = {*(const unsigned*)(shm + (ak2 - lb)), 0u};
      const f32x16 z = {0};
      float ma = max16(mfma32k8(k2, u32x2{q2x[0], 0u}, mfma32(k1, q1[0], z)));
      float mb = max16(mfma32k8(k2, u32x2{q2x[1], 0u}, mfma32(k1, q1[1], z)));
      ma = fmaxf(ma, xhalf(ma, h)) + (float)96;
      mb = fmaxf(mb, xhalf(mb, h)) + (float)96;
      unsigned xa = h ? (pk2(-ma, 0.0f) & 0xffffu) : q2x[0], xb = h ? (pk2(-mb, 0.0f) & 0xffffu) : q2x[1];
      q2a = u32x4{xa, 0u, 0u, xa};
      q2b = u32x4{xb, 0u, 0u, xb};
    }
    u32x4 sk = {WS_KV + 40960 + w * 5120, lb + 40960 + w * 5120, wb + 12 * 1024, WS_WOS + ct * 18432 + w * 5120};
    {
      const u32x4 z = {~0u, ~0u, ~0u, ~0u};
      st16(rw, obase + 2 * w * 1024 + vl, z); st16(rw, obase + (2 * w + 1) * 1024 + vl, z);
      if (w == 3) st16(rw, obase + 8192 + vl, z);
    }
    const u32x4 tg4 = {(unsigned)tag, (unsigned)(tag >> 32), (unsigned)tag, (unsigned)(tag >> 32)};
    const unsigned fsv = threadIdx.x == 0 ? C_FO + (R * 8 + g8) * 128 : 0x7ffffff0u;
    f32x16 oa, ob;
    asm volatile(
    "ds_read_b128 v[112:115], v145\n"
    "ds_read_b128 v[120:123], v146\n"
    "ds_read_b128 v[128:131], v147\n"
    "ds_read_b128 v[132:135], v147 offset:608\n"
    "ds_read_b128 v[116:119], v145 offset:2240\n"
    "s_setprio 1\n"
    "v_mov_b32 v144, 0xdddddddd\n"
    "v_mov_b32 v48, 0\n"
    "v_mov_b32 v49, 0\n"
    "v_mov_b32 v50, 0\n"
    "v_mov_b32 v51, 0\n"
    "v_mov_b32 v52, 0\n"
    "v_mov_b32 v53, 0\n"
    "v_mov_b32 v54, 0\n"
    "v_mov_b32 v55, 0\n"
    "v_mov_b32 v56, 0\n"
    "v_mov_b32 v57, 0\n"
    "v_mov_b32 v58, 0\n"
    "v_mov_b32 v59, 0\n"
    "v_mov_b32 v60, 0\n"
    "v_mov_b32 v61, 0\n"
    "v_mov_b32 v62, 0\n"
    "v_mov_b32 v63, 0\n"
    "v_mov_b32 v64, 0\n"
    "v_mov_b32 v65, 0\n"
    "v_mov_b32 v66, 0\n"
    "v_mov_b32 v67, 0\n"
    "v_mov_b32 v68, 0\n"
    "v_mov_b32 v69, 0\n"
    "v_mov_b32 v70, 0\n"
    "v_mov_b32 v71, 0\n"
    "v_mov_b32 v72, 0\n"
    "v_mov_b32 v73, 0\n"
    "v_mov_b32 v74, 0\n"
    "v_mov_b32 v75, 0\n"
    "v_mov_b32 v76, 0\n"
    "v_mov_b32 v77, 0\n"
    "v_mov_b32 v78, 0\n"
    "v_mov_b32 v79, 0\n"
    "v_mov_b32 v80, 0\n"
    "v_mov_b32 v81, 0\n"
    "v_mov_b32 v82, 0\n"
    "v_mov_b32 v83, 0\n"
    "v_mov_b32 v84, 0\n"
    "v_mov_b32 v85, 0\n"
    "v_mov_b32 v86, 0\n"
    "v_mov_b32 v87, 0\n"
    "v_mov_b32 v88, 0\n"
    "v_mov_b32 v89, 0\n"
    "v_mov_b32 v90, 0\n"
    "v_mov_b32 v91, 0\n"
    "v_mov_b32 v92, 0\n"
    "v_mov_b32 v93, 0\n"
    "v_mov_b32 v94, 0\n"
    "v_mov_b32 v95, 0\n"
    "v_mov_b32 v136, 0\n"
    "v_mov_b32 v137, 0\n"
    "v_mov_b32 v138, 0\n"
    "v_mov_b32 v139, 0\n"
    "v_mov_b32 v140, 0\n"
    "v_mov_b32 v141, 0\n"
    "v_mov_b32 v142, 0\n"
    "v_mov_b32 v143, 0\n"
    "s_mov_b32 s60, 0\n"
    "s_waitcnt lgkmcnt(0)\n"
    "v_mfma_f32_32x32x16_bf16 v[0:15], v[112:115], v[96:99], 0\n"
    "v_mfma_f32_32x32x8_bf16 v[0:15], v[120:121], v[104:105], v[0:15]\n"
    "s_nop 15\n"
    "LATT_%=:\n"
    "v_mfma_f32_32x32x16_bf16 v[16:31], v[112:115], v[100:103], 0\n"
    "v_exp_f32 v0, v0\n"
    "v_exp_f32 v1, v1\n"
    "v_exp_f32 v2, v2\n"
    "v_mfma_f32_32x32x8_bf16 v[16:31], v[120:121], v[108:109], v[16:31]\n"
    "v_exp_f32 v3, v3\n"
    "v_exp_f32 v4, v4\n"
    "v_exp_f32 v5, v5\n"
    "v_exp_f32 v6, v6\n"
    "v_exp_f32 v7, v7\n"
    "v_smfmac_f32_32x32x32_bf16 v[80:95], v[136:139], v[48:55], v144\n"
    "v_exp_f32 v8, v8\n"
    "v_exp_f32 v9, v9\n"
    "v_smfmac_f32_32x32x32_bf16 v[80:95], v[140:143], v[56:63], v144\n"
    "s_mov_b32 m0, s41\n"
    "s_add_u32 s61, s40, 0x1000\n"
    "buffer_load_dwordx4 v148, s[36:39], s40 offen offset:0 sc1 lds\n"
    "v_exp_f32 v10, v10\n"
    "v_exp_f32 v11, v11\n"
    "v_exp_f32 v12, v12\n"
    "v_exp_f32 v13, v13\n"
    "v_exp_f32 v14, v14\n"
    "v_exp_f32 v15, v15\n"
    "s_waitcnt lgkmcnt(0)\n"
    "v_mfma_f32_32x32x16_bf16 v[32:47], v[116:119], v[96:99], 0\n"
    "v_exp_f32 v16, v16\n"
    "v_exp_f32 v17, v17\n"
    "v_exp_f32 v18, v18\n"
    "v_mfma_f32_32x32x8_bf16 v[32:47], v[120:121], v[106:107], v[32:47]\n"
    "ds_read_b128 v[136:139], v147 offset:2240\n"
    "ds_read_b128 v[140:143], v147 offset:2848\n"
    "v_exp_f32 v19, v19\n"
    "v_exp_f32 v20, v20\n"
    "v_exp_f32 v21, v21\n"
    "v_exp_f32 v22, v22\n"
    "v_exp_f32 v23, v23\n"
    "v_smfmac_f32_32x32x32_bf16 v[64:79], v[128:131], v[0:7], v144\n"
    "v_exp_f32 v24, v24\n"
    "v_exp_f32 v25, v25\n"
    "v_smfmac_f32_32x32x32_bf16 v[64:79], v[132:135], v[8:15], v144\n"
    "ds_read_b128 v[112:115], v145 offset:4480\n"
    "buffer_load_dwordx4 v148, s[36:39], s40 offen offset:1024 sc1 lds\n"
    "v_exp_f32 v26, v26\n"
    "v_exp_f32 v27, v27\n"
    "v_exp_f32 v28, v28\n"
    "v_exp_f32 v29, v29\n"
    "v_exp_f32 v30, v30\n"
    "v_exp_f32 v31, v31\n"
    "v_mfma_f32_32x32x16_bf16 v[48:63], v[116:119], v[100:103], 0\n"
    "v_exp_f32 v32, v32\n"
    "v_exp_f32 v33, v33\n"
    "v_exp_f32 v34, v34\n"
    "v_mfma_f32_32x32x8_bf16 v[48:63], v[120:121], v[110:111], v[48:63]\n"
    "v_exp_f32 v35, v35\n"
    "v_exp_f32 v36, v36\n"
    "v_exp_f32 v37, v37\n"
    "v_exp_f32 v38, v38\n"
    "v_exp_f32 v39, v39\n"
    "v_smfmac_f32_32x32x32_bf16 v[80:95], v[128:131], v[16:23], v144\n"
    "v_exp_f32 v40, v40\n"
    "v_exp_f32 v41, v41\n"
    "v_smfmac_f32_32x32x32_bf16 v[80:95], v[132:135], v[24:31], v144\n"
    "buffer_load_dwordx4 v148, s[36:39], s40 offen offset:2048 sc1 lds\n"
    "v_exp_f32 v42, v42\n"
    "v_exp_f32 v43, v43\n"
    "v_exp_f32 v44, v44\n"
    "v_exp_f32 v45, v45\n"
    "v_exp_f32 v46, v46\n"
    "v_exp_f32 v47, v47\n"
    "s_waitcnt lgkmcnt(0)\n"
    "v_mfma_f32_32x32x16_bf16 v[0:15], v[112:115], v[96:99], 0\n"
    "v_exp_f32 v48, v48\n"
    "v_exp_f32 v49, v49\n"
    "v_exp_f32 v50, v50\n"
    "v_mfma_f32_32x32x8_bf16 v[0:15], v[122:123], v[104:105], v[0:15]\n"
    "ds_read_b128 v[128:131], v147 offset:4480\n"
    "ds_read_b128 v[132:135], v147 offset:5088\n"
    "v_exp_f32 v51, v51\n"
    "v_exp_f32 v52, v52\n"
    "v_exp_f32 v53, v53\n"
    "v_exp_f32 v54, v54\n"
    "v_exp_f32 v55, v55\n"
    "v_smfmac_f32_32x32x32_bf16 v[64:79], v[136:139], v[32:39], v144\n"
    "v_exp_f32 v56, v56\n"
    "v_exp_f32 v57, v57\n"
    "v_smfmac_f32_32x32x32_bf16 v[64:79], v[140:143], v[40:47], v144\n"
    "ds_read_b128 v[116:119], v145 offset:6720\n"
    "buffer_load_dwordx4 v148, s[36:39], s40 offen offset:3072 sc1 lds\n"
    "v_exp_f32 v58, v58\n"
    "v_exp_f32 v59, v59\n"
    "v_exp_f32 v60, v60\n"
    "v_exp_f32 v61, v61\n"
    "v_exp_f32 v62, v62\n"
    "v_exp_f32 v63, v63\n"
    "v_mfma_f32_32x32x16_bf16 v[16:31], v[112:115], v[100:103], 0\n"
    "v_exp_f32 v0, v0\n"
    "v_exp_f32 v1, v1\n"
    "v_exp_f32 v2, v2\n"
    "v_mfma_f32_32x32x8_bf16 v[16:31], v[122:123], v[108:109], v[16:31]\n"
    "v_exp_f32 v3, v3\n"
    "v_exp_f32 v4, v4\n"
    "v_exp_f32 v5, v5\n"
    "v_exp_f32 v6, v6\n"
    "v_exp_f32 v7, v7\n"
    "v_smfmac_f32_32x32x32_bf16 v[80:95], v[136:139], v[48:55], v144\n"
    "v_exp_f32 v8, v8\n"
    "v_exp_f32 v9, v9\n"
    "v_smfmac_f32_32x32x32_bf16 v[80:95], v[140:143], v[56:63], v144\n"
    "s_add_u32 m0, s41, 0x1000\n"
    "s_add_u32 s40, s40, 20480\n"
    "buffer_load_dwordx4 v148, s[36:39], s61 offen sc1 lds\n"
    "v_exp_f32 v10, v10\n"
    "v_exp_f32 v11, v11\n"
    "v_exp_f32 v12, v12\n"
    "v_exp_f32 v13, v13\n"
    "v_exp_f32 v14, v14\n"
    "v_exp_f32 v15, v15\n"
    "s_waitcnt lgkmcnt(0)\n"
    "v_mfma_f32_32x32x16_bf16 v[32:47], v[116:119], v[96:99], 0\n"
    "v_exp_f32 v16, v16\n"
    "v_exp_f32 v17, v17\n"
    "v_exp_f32 v18, v18\n"
    "v_mfma_f32_32x32x8_bf16 v[32:47], v[122:123], v[106:107], v[32:47]\n"
    "ds_read_b128 v[136:139], v147 offset:6720\n"
    "ds_read_b128 v[140:143], v147 offset:7328\n"
    "v_exp_f32 v19, v19\n"
    "v_exp_f32 v20, v20\n"
    "v_exp_f32 v21, v21\n"
    "v_exp_f32 v22, v22\n"
    "v_exp_f32 v23, v23\n"
    "v_smfmac_f32_32x32x32_bf16 v[64:79], v[128:131], v[0:7], v144\n"
    "v_exp_f32 v24, v24\n"
    "v_exp_f32 v25, v25\n"
    "v_smfmac_f32_32x32x32_bf16 v[64:79], v[132:135], v[8:15], v144\n"
    "ds_read_b128 v[112:115], v145 offset:8960\n"
    "ds_read_b128 v[124:127], v146 offset:528\n"
    "v_exp_f32 v26, v26\n"
    "v_exp_f32 v27, v27\n"
    "v_exp_f32 v28, v28\n"
    "v_exp_f32 v29, v29\n"
    "v_exp_f32 v30, v30\n"
    "v_exp_f32 v31, v31\n"
    "v_mfma_f32_32x32x16_bf16 v[48:63], v[116:119], v[100:103], 0\n"
    "v_exp_f32 v32, v32\n"
    "v_exp_f32 v33, v33\n"
    "v_exp_f32 v34, v34\n"
    "v_mfma_f32_32x32x8_bf16 v[48:63], v[122:123], v[110:111], v[48:63]\n"
    "v_exp_f32 v35, v35\n"
    "v_exp_f32 v36, v36\n"
    "v_exp_f32 v37, v37\n"
    "v_exp_f32 v38, v38\n"
    "v_exp_f32 v39, v39\n"
    "v_smfmac_f32_32x32x32_bf16 v[80:95], v[128:131], v[16:23], v144\n"
    "v_exp_f32 v40, v40\n"
    "v_exp_f32 v41, v41\n"
    "v_smfmac_f32_32x32x32_bf16 v[80:95], v[132:135], v[24:31], v144\n"
    "s_cmp_eq_u32 s60, 5\n"
    "s_cselect_b32 s40, s42, s40\n"
    "s_cmp_eq_u32 s60, 6\n"
    "s_cselect_b32 s40, s43, s40\n"
    "s_add_u32 s41, s41, 20480\n"
    "s_sub_u32 s61, s41, 81920\n"
    "s_cselect_b32 s41, s41, s61\n"
    "v_exp_f32 v42, v42\n"
    "v_exp_f32 v43, v43\n"
    "v_exp_f32 v44, v44\n"
    "v_exp_f32 v45, v45\n"
    "v_exp_f32 v46, v46\n"
    "v_exp_f32 v47, v47\n"
    "s_waitcnt lgkmcnt(0)\n"
    "v_mfma_f32_32x32x16_bf16 v[0:15], v[112:115], v[96:99], 0\n"
    "v_exp_f32 v48, v48\n"
    "v_exp_f32 v49, v49\n"
    "v_exp_f32 v50, v50\n"
    "v_mfma_f32_32x32x8_bf16 v[0:15], v[124:125], v[104:105], v[0:15]\n"
    "ds_read_b128 v[128:131], v147 offset:8960\n"
    "ds_read_b128 v[132:135], v147 offset:9568\n"
    "v_exp_f32 v51, v51\n"
    "v_exp_f32 v52, v52\n"
    "v_exp_f32 v53, v53\n"
    "v_exp_f32 v54, v54\n"
    "v_exp_f32 v55, v55\n"
    "v_smfmac_f32_32x32x32_bf16 v[64:79], v[136:139], v[32:39], v144\n"
    "v_exp_f32 v56, v56\n"
    "v_exp_f32 v57, v57\n"
    "v_smfmac_f32_32x32x32_bf16 v[64:79], v[140:143], v[40:47], v144\n"
    "ds_read_b128 v[116:119], v145 offset:11200\n"
    "v_exp_f32 v58, v58\n"
    "v_exp_f32 v59, v59\n"
    "v_exp_f32 v60, v60\n"
    "v_exp_f32 v61, v61\n"
    "v_exp_f32 v62, v62\n"
    "v_exp_f32 v63, v63\n"
    "v_mfma_f32_32x32x16_bf16 v[16:31], v[112:115], v[100:103], 0\n"
    "v_exp_f32 v0, v0\n"
    "v_exp_f32 v1, v1\n"
    "v_exp_f32 v2, v2\n"
    "v_mfma_f32_32x32x8_bf16 v[16:31], v[124:125], v[108:109], v[16:31]\n"
    "v_exp_f32 v3, v3\n"
    "v_exp_f32 v4, v4\n"
    "v_exp_f32 v5, v5\n"
    "v_exp_f32 v6, v6\n"
    "v_exp_f32 v7, v7\n"
    "v_smfmac_f32_32x32x32_bf16 v[80:95], v[136:139], v[48:55], v144\n"
    "v_exp_f32 v8, v8\n"
    "v_exp_f32 v9, v9\n"
    "v_smfmac_f32_32x32x32_bf16 v[80:95], v[140:143], v[56:63], v144\n"
    "v_exp_f32 v10, v10\n"
    "v_exp_f32 v11, v11\n"
    "v_exp_f32 v12, v12\n"
    "v_exp_f32 v13, v13\n"
    "v_exp_f32 v14, v14\n"
    "v_exp_f32 v15, v15\n"
    "s_waitcnt lgkmcnt(0)\n"
    "v_mfma_f32_32x32x16_bf16 v[32:47], v[116:119], v[96:99], 0\n"
    "v_exp_f32 v16, v16\n"
    "v_exp_f32 v17, v17\n"
    "v_exp_f32 v18, v18\n"
    "v_mfma_f32_32x32x8_bf16 v[32:47], v[124:125], v[106:107], v[32:47]\n"
    "ds_read_b128 v[136:139], v147 offset:11200\n"
    "ds_read_b128 v[140:143], v147 offset:11808\n"
    "v_exp_f32 v19, v19\n"
    "v_exp_f32 v20, v20\n"
    "v_exp_f32 v21, v21\n"
    "v_exp_f32 v22, v22\n"
    "v_exp_f32 v23, v23\n"
    "v_smfmac_f32_32x32x32_bf16 v[64:79], v[128:131], v[0:7], v144\n"
    "v_exp_f32 v24, v24\n"
    "v_exp_f32 v25, v25\n"
    "v_smfmac_f32_32x32x32_bf16 v[64:79], v[132:135], v[8:15], v144\n"
    "ds_read_b128 v[112:115], v145 offset:13440\n"
    "v_exp_f32 v26, v26\n"
    "v_exp_f32 v27, v27\n"
    "v_exp_f32 v28, v28\n"
    "v_exp_f32 v29, v29\n"
    "v_exp_f32 v30, v30\n"
    "v_exp_f32 v31, v31\n"
    "v_mfma_f32_32x32x16_bf16 v[48:63], v[116:119], v[100:103], 0\n"
    "v_exp_f32 v32, v32\n"
    "v_exp_f32 v33, v33\n"
    "v_exp_f32 v34, v34\n"
    "v_mfma_f32_32x32x8_bf16 v[48:63], v[124:125], v[110:111], v[48:63]\n"
    "v_exp_f32 v35, v35\n"
    "v_exp_f32 v36, v36\n"
    "v_exp_f32 v37, v37\n"
    "v_exp_f32 v38, v38\n"
    "v_exp_f32 v39, v39\n"
    "v_smfmac_f32_32x32x32_bf16 v[80:95], v[128:131], v[16:23], v144\n"
    "v_exp_f32 v40, v40\n"
    "v_exp_f32 v41, v41\n"
    "v_smfmac_f32_32x32x32_bf16 v[80:95], v[132:135], v[24:31], v144\n"
    "v_exp_f32 v42, v42\n"
    "v_exp_f32 v43, v43\n"
    "v_exp_f32 v44, v44\n"
    "v_exp_f32 v45, v45\n"
    "v_exp_f32 v46, v46\n"
    "v_exp_f32 v47, v47\n"
    "s_waitcnt lgkmcnt(0)\n"
    "v_mfma_f32_32x32x16_bf16 v[0:15], v[112:115], v[96:99], 0\n"
    "v_exp_f32 v48, v48\n"
    "v_exp_f32 v49, v49\n"
    "v_exp_f32 v50, v50\n"
    "v_mfma_f32_32x32x8_bf16 v[0:15], v[126:127], v[104:105], v[0:15]\n"
    "ds_read_b128 v[128:131], v147 offset:13440\n"
    "ds_read_b128 v[132:135], v147 offset:14048\n"
    "v_exp_f32 v51, v51\n"
    "v_exp_f32 v52, v52\n"
    "v_exp_f32 v53, v53\n"
    "v_exp_f32 v54, v54\n"
    "v_exp_f32 v55, v55\n"
    "v_smfmac_f32_32x32x32_bf16 v[64:79], v[136:139], v[32:39], v144\n"
    "v_exp_f32 v56, v56\n"
    "v_exp_f32 v57, v57\n"
    "v_smfmac_f32_32x32x32_bf16 v[64:79], v[140:143], v[40:47], v144\n"
    "ds_read_b128 v[116:119], v145 offset:15680\n"
    "v_exp_f32 v58, v58\n"
    "v_exp_f32 v59, v59\n"
    "v_exp_f32 v60, v60\n"
    "v_exp_f32 v61, v61\n"
    "v_exp_f32 v62, v62\n"
    "v_exp_f32 v63, v63\n"
    "v_mfma_f32_32x32x16_bf16 v[16:31], v[112:115], v[100:103], 0\n"
    "v_exp_f32 v0, v0\n"
    "v_exp_f32 v1, v1\n"
    "v_exp_f32 v2, v2\n"
    "v_mfma_f32_32x32x8_bf16 v[16:31], v[126:127], v[108:109], v[16:31]\n"
    "v_exp_f32 v3, v3\n"
    "v_exp_f32 v4, v4\n"
    "v_exp_f32 v5, v5\n"
    "v_exp_f32 v6, v6\n"
    "v_exp_f32 v7, v7\n"
    "v_smfmac_f32_32x32x32_bf16 v[80:95], v[136:139], v[48:55], v144\n"
    "v_exp_f32 v8, v8\n"
    "v_exp_f32 v9, v9\n"
    "v_smfmac_f32_32x32x32_bf16 v[80:95], v[140:143], v[56:63], v144\n"
    "v_exp_f32 v10, v10\n"
    "v_exp_f32 v11, v11\n"
    "v_exp_f32 v12, v12\n"
    "v_exp_f32 v13, v13\n"
    "v_exp_f32 v14, v14\n"
    "v_exp_f32 v15, v15\n"
    "s_waitcnt lgkmcnt(0)\n"
    "v_mfma_f32_32x32x16_bf16 v[32:47], v[116:119], v[96:99], 0\n"
    "v_exp_f32 v16, v16\n"
    "v_exp_f32 v17, v17\n"
    "v_exp_f32 v18, v18\n"
    "v_mfma_f32_32x32x8_bf16 v[32:47], v[126:127], v[106:107], v[32:47]\n"
    "ds_read_b128 v[136:139], v147 offset:15680\n"
    "ds_read_b128 v[140:143], v147 offset:16288\n"
    "v_exp_f32 v19, v19\n"
    "v_exp_f32 v20, v20\n"
    "v_exp_f32 v21, v21\n"
    "v_exp_f32 v22, v22\n"
    "v_exp_f32 v23, v23\n"
    "v_smfmac_f32_32x32x32_bf16 v[64:79], v[128:131], v[0:7], v144\n"
    "v_exp_f32 v24, v24\n"
    "v_exp_f32 v25, v25\n"
    "v_smfmac_f32_32x32x32_bf16 v[64:79], v[132:135], v[8:15], v144\n"
    "s_and_b32 s61, s60, 3\n"
    "s_cmp_eq_u32 s61, 3\n"
    "s_mov_b32 s61, 20480\n"
    "s_cselect_b32 s61, -61440, s61\n"
    "v_add_u32 v145, s61, v145\n"
    "v_add_u32 v146, s61, v146\n"
    "v_add_u32 v147, s61, v147\n"
    "s_bitcmp1_b32 0x2a, s60\n"
    "s_cbranch_scc0 LATB_%=\n"
    "s_waitcnt vmcnt(0) lgkmcnt(0)\n"
    "s_barrier\n"
    "buffer_store_dwordx4 v[150:153], v149, s[36:39], 0 offen sc1\n"
    "LATB_%=:\n"
    "ds_read_b128 v[112:115], v145 offset:0\n"
    "ds_read_b128 v[120:123], v146 offset:0\n"
    "v_exp_f32 v26, v26\n"
    "v_exp_f32 v27, v27\n"
    "v_exp_f32 v28, v28\n"
    "v_exp_f32 v29, v29\n"
    "v_exp_f32 v30, v30\n"
    "v_exp_f32 v31, v31\n"
    "v_mfma_f32_32x32x16_bf16 v[48:63], v[116:119], v[100:103], 0\n"
    "v_exp_f32 v32, v32\n"
    "v_exp_f32 v33, v33\n"
    "v_exp_f32 v34, v34\n"
    "v_mfma_f32_32x32x8_bf16 v[48:63], v[126:127], v[110:111], v[48:63]\n"
    "v_exp_f32 v35, v35\n"
    "v_exp_f32 v36, v36\n"
    "v_exp_f32 v37, v37\n"
    "v_exp_f32 v38, v38\n"
    "v_exp_f32 v39, v39\n"
    "v_smfmac_f32_32x32x32_bf16 v[80:95], v[128:131], v[16:23], v144\n"
    "v_exp_f32 v40, v40\n"
    "v_exp_f32 v41, v41\n"
    "v_smfmac_f32_32x32x32_bf16 v[80:95], v[132:135], v[24:31], v144\n"
    "v_exp_f32 v42, v42\n"
    "v_exp_f32 v43, v43\n"
    "v_exp_f32 v44, v44\n"
    "v_exp_f32 v45, v45\n"
    "v_exp_f32 v46, v46\n"
    "v_exp_f32 v47, v47\n"
    "s_waitcnt lgkmcnt(0)\n"
    "v_mfma_f32_32x32x16_bf16 v[0:15], v[112:115], v[96:99], 0\n"
    "v_exp_f32 v48, v48\n"
    "v_exp_f32 v49, v49\n"
    "v_exp_f32 v50, v50\n"
    "v_mfma_f32_32x32x8_bf16 v[0:15], v[120:121], v[104:105], v[0:15]\n"
    "ds_read_b128 v[128:131], v147 offset:0\n"
    "ds_read_b128 v[132:135], v147 offset:608\n"
    "v_exp_f32 v51, v51\n"
    "v_exp_f32 v52, v52\n"
    "v_exp_f32 v53, v53\n"
    "v_exp_f32 v54, v54\n"
    "v_exp_f32 v55, v55\n"
    "v_smfmac_f32_32x32x32_bf16 v[64:79], v[136:139], v[32:39], v144\n"
    "v_exp_f32 v56, v56\n"
    "v_exp_f32 v57, v57\n"
    "v_smfmac_f32_32x32x32_bf16 v[64:79], v[140:143], v[40:47], v144\n"
    "ds_read_b128 v[116:119], v145 offset:2240\n"
    "v_exp_f32 v58, v58\n"
    "v_exp_f32 v59, v59\n"
    "v_exp_f32 v60, v60\n"
    "v_exp_f32 v61, v61\n"
    "v_exp_f32 v62, v62\n"
    "v_exp_f32 v63, v63\n"
    "s_add_u32 s60, s60, 1\n"
    "s_cmp_lg_u32 s60, 8\n"
    "s_cbranch_scc1 LATT_%=\n"
    "s_setprio 3\n"
    "s_nop 0\n"
    "v_smfmac_f32_32x32x32_bf16 v[80:95], v[136:139], v[48:55], v144\n"
    "v_smfmac_f32_32x32x32_bf16 v[80:95], v[140:143], v[56:63], v144\n"
    "s_waitcnt lgkmcnt(0)\n"
    "s_nop 15\n"
    "s_nop 3\n"
        : "={v[64:79]}"(oa), "={v[80:95]}"(ob), "+{v145}"(ak1), "+{v146}"(ak2), "+{v147}"(av), "+{s[40:43]}"(sk)
        : "{v[96:99]}"(q1[0]), "{v[100:103]}"(q1[1]), "{v[104:107]}"(q2a), "{v[108:111]}"(q2b), "{v148}"(vl), "{s[36:39]}"(srdK), "{s44}"(w), "{v149}"(fsv), "{v[150:153]}"(tg4)
        : "v0", "v1", "v2", "v3", "v4", "v5", "v6", "v7", "v8", "v9", "v10", "v11", "v12", "v13", "v14", "v15", "v16", "v17", "v18", "v19", "v20", "v21", "v22", "v23", "v24", "v25", "v26", "v27", "v28", "v29", "v30", "v31", "v32", "v33", "v34", "v35", "v36", "v37", "v38", "v39", "v40", "v41", "v42", "v43", "v44", "v45", "v46", "v47", "v48", "v49", "v50", "v51", "v52", "v53", "v54", "v55", "v56", "v57", "v58", "v59", "v60", "v61", "v62", "v63", "v112", "v113", "v114", "v115", "v116", "v117", "v118", "v119", "v120", "v121", "v122", "v123", "v124", "v125", "v126", "v127", "v128", "v129", "v130", "v131", "v132", "v133", "v134", "v135", "v136", "v137", "v138", "v139", "v140", "v141", "v142", "v143", "v144", "s60", "s61", "scc", "memory");
    const unsigned fso = C_FO + (R * 8 + 2 * w + (lane & 1)) * 128;
    const u32x4 fs = __builtin_amdgcn_raw_buffer_load_b128(rw, fso, 0, 16);
    if (__builtin_amdgcn_ballot_w64(att_bad(oa, h))) oa = att_exact(rw, q1[0], q2x[0], lane);
    if (__builtin_amdgcn_ballot_w64(att_bad(ob, h))) ob = att_exact(rw, q1[1], q2x[1], lane);
#pragma unroll
    for (int g = 0; g < 2; ++g) {
      const f32x16& o = g ? ob : oa;
      float lo = xhalf(o[10], h);
      float inv = __builtin_amdgcn_rcpf(h ? lo : o[10]);
      u32x4 v = {pk2s(o[0] * inv, o[1] * inv), pk2s(o[2] * inv, o[3] * inv), pk2s(o[4] * inv, o[5] * inv), pk2s(o[6] * inv, o[7] * inv)};
      st16(rw, obase + (32 * g + r) * 16 + (2 * w + h) * 1024, v);
      if (h == 0) st4(rw, obase + (32 * g + r) * 16 + 8192 + w * 4, pk2s(o[8] * inv, o[9] * inv));
    }
    {
#pragma unroll
      for (int ks = 12 + 5; ks < 36; ++ks) wf[ks] = __builtin_amdgcn_raw_buffer_load_b128(rw, vl + (ks & 3) * 1024, wb + (ks >> 2) * 4096, 16);
      SB();
      BARRIER();
      if (__builtin_amdgcn_ballot_w64(!flags_ok(fs, tag))) wait_flags(rw, fso, tag);
      __builtin_amdgcn_s_sleep(12);
      u32x4 pv[9];
      const unsigned vp = WS_O + R * 73728 + w * 18432 + h * 1024 + (32 * rh + r) * 16;
#pragma unroll 1
      for (unsigned spins = 0; spins < (1u << 18); ++spins) {
        asm volatile("" ::: "memory");
        unsigned mx = 0;
#pragma unroll
        for (int n = 0; n < 9; ++n) pv[n] = __builtin_amdgcn_raw_buffer_load_b128(rw, vp, n * 2048, 16);
#pragma unroll
        for (int n = 0; n < 9; ++n)
#pragma unroll
          for (int e = 0; e < 4; ++e) mx = pv[n][e] > mx ? pv[n][e] : mx;
        if (__builtin_amdgcn_ballot_w64(mx == ~0u) == 0) break;
        __builtin_amdgcn_s_sleep(2);
      }
      WAITV(0);
#pragma unroll
      for (int n = 0; n < 9; ++n) *(u32x4*)(shm + 40960 + (9 * w + n) * 1024 + vl) = pv[n];
#pragma unroll
      for (int ks = 12; ks < 12 + 5; ++ks) wf[ks] = *(const u32x4*)(shm + w * 5120 + (ks - 12) * 1024 + vl);
      BARRIER();
      const unsigned o = (R * 64 + rh * 32) * 2304 + ct * 288;
      f32x16 acc = {0};
      u32x4 pf[6], sp[9], sw[9];
      const char* spp = shm + 40960 + (w >> 1) * 18432 + (lane >> 4) * 512 + (16 * (w & 1) + (lane & 15)) * 16;
      const char* swp = shm + 20480 + (w >> 1) * 9216 + vl;
#pragma unroll
      for (int ks = 0; ks < 6; ++ks) pf[ks] = *(const u32x4*)(shm + 40960 + ks * 1024 + vl);
      f32x4 accs = {0};
#pragma unroll
      for (int ks = 0; ks < 36; ++ks) {
        SB(); acc = mfma32(wf[ks], pf[ks % 6], acc);
        SB(); if (ks + 6 < 36) pf[ks % 6] = *(const u32x4*)(shm + 40960 + (ks + 6) * 1024 + vl);
        if (ks < 9) { sp[ks] = *(const u32x4*)(spp + ks * 2048); sw[ks] = *(const u32x4*)(swp + ks * 1024); }
        if (ks >= 4 && ks < 13) { SB(); accs = mfma16(sw[ks - 4], sp[ks - 4], accs); }
        if (ks == 20 && w >= 2) *(f32x4*)(shm + (w & 1) * 1024 + vl) = accs;
      }
      SB();
#pragma unroll
      for (int u = 0; u < 2; ++u) __builtin_amdgcn_raw_buffer_store_b128(pack_half(acc, u, 1.0f), ro, o + r * 2304 + (32 * w + 8 * (2 * u + h)) * 2, 0, 0);
      BARRIER();
      if (w < 2) {
        accs += *(const f32x4*)(shm + w * 1024 + vl);
        __builtin_amdgcn_raw_buffer_store_b64(u32x2{pk2(accs[0], accs[1]), pk2(accs[2], accs[3])}, ro, o + (16 * w + (lane & 15)) * 2304 + (128 + 4 * (lane >> 4)) * 2, 0, 0);
      }
    }
    if (blockIdx.x == 0 && w == 3) {
#pragma unroll 1
      for (int i = 0; i < 4; ++i) wait_flags(rw, C_FO + (i * 64 + lane) * 128, tag);
      if (lane == 0) set_flag(rw, C_E, tag);
    }
    return;
  }
  __builtin_amdgcn_s_setprio(3);
  u64 tag;
  const int xoff = r * 128 + ((h ^ ((r >> 1) & 7)) << 4);
  const int rl = lane >> 3;
  const unsigned sw0 = ((lane & 7) ^ (rl >> 1)) << 4, sw1 = ((lane & 7) ^ (4 + (rl >> 1))) << 4;
  const unsigned vx0 = (16 * w + rl) * 1152 + sw0, vx1 = (16 * w + 8 + rl) * 1152 + sw1;
  const unsigned vw0 = (128 * (g8 & 3) + 32 * w + rl) * 1152 + sw0, vw1 = (128 * (g8 & 3) + 32 * w + 8 + rl) * 1152 + sw1;
  const unsigned vws = (512 + 16 * (g8 & 3) + 8 * (w & 1) + rl) * 1152 + ((w & 1) ? sw1 : sw0);
  const u32x4 srdBX = mk_srd(Bx + R * 73728, 73728u), srdBW = mk_srd((g8 >> 2) ? Wdim : Wdre, 663552u);
  auto piece = [&](int k, int slot, int i) {
    unsigned d = lb + slot * 26624;
    k += rot; k = k >= 9 ? k - 9 : k;
    if (i == 0) glds<false>(srdBX, vx0, k * 128, d + w * 2048);
    if (i == 1) glds<false>(srdBX, vx1, k * 128, d + w * 2048 + 1024);
    if (i == 2) glds<false>(srdBW, vw0, k * 128, d + 8192 + w * 4096);
    if (i == 3) glds<false>(srdBW, vw1, k * 128, d + 8192 + w * 4096 + 1024);
    if (i == 4) glds<false>(srdBW, vw0, k * 128 + 18432, d + 8192 + w * 4096 + 2048);
    if (i == 5) glds<false>(srdBW, vw1, k * 128 + 18432, d + 8192 + w * 4096 + 3072);
    if (i == 6 && w < 2) glds<false>(srdBW, vws, k * 128, d + 24576 + w * 1024);
  };
  auto stage = [&](int k) {
    piece(k, k, 6);
#pragma unroll
    for (int i = 0; i < 6; ++i) piece(k, k, i);
  };
  const bool kvjob = g8 < 4;
  const int jg = g8 & 3;
  if (kvjob) {
    const u32x4 srdX = mk_srd(Ax + R * 73728 + (jg >> 1) * 36864, 36864u), srdW = mk_srd((jg & 1) ? wv : wk, 20736u);
#pragma unroll
    for (int n = 0; n < 6; ++n) glds<false>(srdW, vl + w * 5184 + n * 1024, 0, lb + w * 15360 + n * 1024);
#pragma unroll
    for (int n = 0; n < 9; ++n) { int idx = n * 64 + lane; glds<false>(srdX, (idx / 18) * 1152 + (idx % 18) * 16, w * 288, lb + w * 15360 + 6144 + n * 1024); }
    const int t = 2 * R + (jg >> 1);
    const unsigned so0 = kv_tile(t) + ((jg & 1) ? v_off(r, h) : vl), so1 = (jg & 1) ? so0 + 608 : kv_k2(t) + vl;
    u32x4 xf[9], wf[9];
    WAITV(9);
    asm volatile(
        "ds_read_u16 v16, %9 offset:0\n"
        "ds_read_u16 v52, %9 offset:36\n"
        "ds_read_u16 v17, %9 offset:72\n"
        "ds_read_u16 v53, %9 offset:108\n"
        "ds_read_u16 v18, %9 offset:144\n"
        "ds_read_u16 v54, %9 offset:180\n"
        "ds_read_u16 v19, %9 offset:216\n"
        "ds_read_u16 v55, %9 offset:252\n"
        "ds_read_u16 v20, %9 offset:576\n"
        "ds_read_u16 v56, %9 offset:612\n"
        "ds_read_u16 v21, %9 offset:648\n"
        "ds_read_u16 v57, %9 offset:684\n"
        "ds_read_u16 v22, %9 offset:720\n"
        "ds_read_u16 v58, %9 offset:756\n"
        "ds_read_u16 v23, %9 offset:792\n"
        "ds_read_u16 v59, %9 offset:828\n"
        "ds_read_u16 v24, %9 offset:1152\n"
        "ds_read_u16 v60, %9 offset:1188\n"
        "ds_read_u16 v25, %9 offset:1224\n"
        "ds_read_u16 v61, %9 offset:1260\n"
        "ds_read_u16 v26, %9 offset:1296\n"
        "ds_read_u16 v62, %9 offset:1332\n"
        "ds_read_u16 v27, %9 offset:1368\n"
        "ds_read_u16 v63, %9 offset:1404\n"
        "ds_read_u16 v28, %9 offset:1728\n"
        "ds_read_u16 v64, %9 offset:1764\n"
        "ds_read_u16 v29, %9 offset:1800\n"
        "ds_read_u16 v65, %9 offset:1836\n"
        "ds_read_u16 v30, %9 offset:1872\n"
        "ds_read_u16 v66, %9 offset:1908\n"
        "ds_read_u16 v31, %9 offset:1944\n"
        "ds_read_u16 v67, %9 offset:1980\n"
        "ds_read_u16 v32, %9 offset:2304\n"
        "ds_read_u16 v68, %9 offset:2340\n"
        "ds_read_u16 v33, %9 offset:2376\n"
        "ds_read_u16 v69, %9 offset:2412\n"
        "ds_read_u16 v34, %9 offset:2448\n"
        "ds_read_u16 v70, %9 offset:2484\n"
        "ds_read_u16 v35, %9 offset:2520\n"
        "ds_read_u16 v71, %9 offset:2556\n"
        "ds_read_u16 v36, %9 offset:2880\n"
        "ds_read_u16 v72, %9 offset:2916\n"
        "ds_read_u16 v37, %9 offset:2952\n"
        "ds_read_u16 v73, %9 offset:2988\n"
        "ds_read_u16 v38, %9 offset:3024\n"
        "ds_read_u16 v74, %9 offset:3060\n"
        "ds_read_u16 v39, %9 offset:3096\n"
        "ds_read_u16 v75, %9 offset:3132\n"
        "ds_read_u16 v40, %9 offset:3456\n"
        "ds_read_u16 v76, %9 offset:3492\n"
        "ds_read_u16 v41, %9 offset:3528\n"
        "ds_read_u16 v77, %9 offset:3564\n"
        "ds_read_u16 v42, %9 offset:3600\n"
        "ds_read_u16 v78, %9 offset:3636\n"
        "ds_read_u16 v43, %9 offset:3672\n"
        "ds_read_u16 v79, %9 offset:3708\n"
        "ds_read_u16 v44, %9 offset:4032\n"
        "ds_read_u16 v80, %9 offset:4068\n"
        "ds_read_u16 v45, %9 offset:4104\n"
        "ds_read_u16 v81, %9 offset:4140\n"
        "ds_read_u16 v46, %9 offset:4176\n"
        "ds_read_u16 v82, %9 offset:4212\n"
        "ds_read_u16 v47, %9 offset:4248\n"
        "ds_read_u16 v83, %9 offset:4284\n"
        "ds_read_u16 v48, %9 offset:4608\n"
        "ds_read_u16 v84, %9 offset:4644\n"
        "ds_read_u16 v49, %9 offset:4680\n"
        "ds_read_u16 v85, %9 offset:4716\n"
        "ds_read_u16 v50, %9 offset:4752\n"
        "ds_read_u16 v86, %9 offset:4788\n"
        "ds_read_u16 v51, %9 offset:4824\n"
        "ds_read_u16 v87, %9 offset:4860\n"
        "s_waitcnt lgkmcnt(0)\n"
        "v_lshl_or_b32 v16, v52, 16, v16\n"
        "v_lshl_or_b32 v17, v53, 16, v17\n"
        "v_lshl_or_b32 v18, v54, 16, v18\n"
        "v_lshl_or_b32 v19, v55, 16, v19\n"
        "v_lshl_or_b32 v20, v56, 16, v20\n"
        "v_lshl_or_b32 v21, v57, 16, v21\n"
        "v_lshl_or_b32 v22, v58, 16, v22\n"
        "v_lshl_or_b32 v23, v59, 16, v23\n"
        "v_lshl_or_b32 v24, v60, 16, v24\n"
        "v_lshl_or_b32 v25, v61, 16, v25\n"
        "v_lshl_or_b32 v26, v62, 16, v26\n"
        "v_lshl_or_b32 v27, v63, 16, v27\n"
        "v_lshl_or_b32 v28, v64, 16, v28\n"
        "v_lshl_or_b32 v29, v65, 16, v29\n"
        "v_lshl_or_b32 v30, v66, 16, v30\n"
        "v_lshl_or_b32 v31, v67, 16, v31\n"
        "v_lshl_or_b32 v32, v68, 16, v32\n"
        "v_lshl_or_b32 v33, v69, 16, v33\n"
        "v_lshl_or_b32 v34, v70, 16, v34\n"
        "v_lshl_or_b32 v35, v71, 16, v35\n"
        "v_lshl_or_b32 v36, v72, 16, v36\n"
        "v_lshl_or_b32 v37, v73, 16, v37\n"
        "v_lshl_or_b32 v38, v74, 16, v38\n"
        "v_lshl_or_b32 v39, v75, 16, v39\n"
        "v_lshl_or_b32 v40, v76, 16, v40\n"
        "v_lshl_or_b32 v41, v77, 16, v41\n"
        "v_lshl_or_b32 v42, v78, 16, v42\n"
        "v_lshl_or_b32 v43, v79, 16, v43\n"
        "v_lshl_or_b32 v44, v80, 16, v44\n"
        "v_lshl_or_b32 v45, v81, 16, v45\n"
        "v_lshl_or_b32 v46, v82, 16, v46\n"
        "v_lshl_or_b32 v47, v83, 16, v47\n"
        "v_lshl_or_b32 v48, v84, 16, v48\n"
        "v_lshl_or_b32 v49, v85, 16, v49\n"
        "v_lshl_or_b32 v50, v86, 16, v50\n"
        "v_lshl_or_b32 v51, v87, 16, v51\n"
        "s_waitcnt vmcnt(0)"
        : "=&{v[16:19]}"(wf[0]), "=&{v[20:23]}"(wf[1]), "=&{v[24:27]}"(wf[2]), "=&{v[28:31]}"(wf[3]), "=&{v[32:35]}"(wf[4]), "=&{v[36:39]}"(wf[5]), "=&{v[40:43]}"(wf[6]), "=&{v[44:47]}"(wf[7]), "=&{v[48:51]}"(wf[8])
        : "v"(lb + w * 15360 + 8 * h * 36 + (r < 18 ? r : 0) * 2)
        : "v52", "v53", "v54", "v55", "v56", "v57", "v58", "v59", "v60", "v61", "v62", "v63", "v64", "v65", "v66", "v67", "v68", "v69", "v70", "v71", "v72", "v73", "v74", "v75", "v76", "v77", "v78", "v79", "v80", "v81", "v82", "v83", "v84", "v85", "v86", "v87", "memory");
#pragma unroll
    for (int k = 0; k < 9; ++k) xf[k] = *(const u32x4*)(shm + w * 15360 + 6144 + r * 288 + h * 16 + k * 32);
    E0USE();
    tag = mk_tag(e0);
    f32x16 c = {0};
    if (jg & 1) {
#pragma unroll
      for (int k = 0; k < 9; ++k) c = mfma32(xf[k], wf[k], c);
    } else {
#pragma unroll
      for (int k = 0; k < 9; ++k) c = mfma32(wf[k], xf[k], c);
    }
    if (w) {
#pragma unroll
      for (int i = 0; i < 4; ++i) *(f32x4*)(shm + 69632 + ((w - 1) * 4 + i) * 1024 + vl) = f32x4{c[4 * i], c[4 * i + 1], c[4 * i + 2], c[4 * i + 3]};
    }
    BARRIER();
    if (w == 0) {
#pragma unroll
      for (int s = 0; s < 3; ++s)
#pragma unroll
        for (int i = 0; i < 4; ++i) {
          f32x4 p = *(const f32x4*)(shm + 69632 + (s * 4 + i) * 1024 + vl);
#pragma unroll
          for (int e = 0; e < 4; ++e) c[4 * i + e] += p[e];
        }
      if (jg & 1) {
        u32x4 v0 = pack_half(c, 0, 1.0f), v1 = pack_half(c, 1, 1.0f);
        if (r >= 18) { unsigned cst = r == 18 ? 0x3f803f80u : 0u; v0 = u32x4{cst, cst, cst, cst}; v1 = v0; }
        if (r < 18 || (r < 20 && h == 0)) { st16(rw, so0, v0); st16(rw, so1, v1); }
      } else {
        st16(rw, so0, u32x4{pk2(c[0], c[1]), pk2(c[2], c[3]), pk2(c[4], c[5]), pk2(c[6], c[7])});
        if (lane <= 32) st4(rw, so1, h ? 0x3f80u : pk2(c[8], c[9]));
      }
      WAITV(0);
      if (lane < 8) st8(rw, C_FKV + lane * 1024 + (R * 4 + jg) * 8, u32x2{(unsigned)tag, (unsigned)(tag >> 32)});
    }
    BARRIER();
  } else {
    const int i = R * 4 + jg, blk = i / 9, grp = i % 9;
    if (R < 18) {
      const u32x4 srdO = mk_srd(wo, 663552u);
#pragma unroll 1
      for (int n = w; n < 9; n += 4) {
        int idx = n * 64 + lane, j = idx / 72, c = idx % 72;
        int k = grp < 8 ? (4 * blk + (grp >> 1)) * 18 + (j & 3) + 8 * (j >> 2) + 4 * (grp & 1) : (4 * blk + (j >> 1)) * 18 + 16 + (j & 1);
        glds<false>(srdO, k * 1152 + c * 16, 0, lb + n * 1024);
      }
    }
    E0USE();
    tag = mk_tag(e0);
    WAITV(0);
    BARRIER();
    if (R < 18) {
#pragma unroll 1
      for (int m = w; m < 9; m += 4) {
        u32x4 f = tr_frag(shm + ((lane & 15) >> 2) * 1152 + m * 128 + (lane >> 4) * 32 + (lane & 3) * 8, 4608);
        const int n = 64 * m + lane, nct = n / 144, nc = n % 144;
        st16(rw, nc < 128 ? WS_WOT + ((nct * 4 + (nc >> 5)) * 36 + (i >> 1)) * 1024 + ((i & 1) * 32 + (nc & 31)) * 16
                          : WS_WOS + (nct * 18 + (i >> 2)) * 1024 + ((i & 3) * 16 + nc - 128) * 16, f);
      }
    }
    WAITV(0);
    BARRIER();
    if (threadIdx.x < 8) st8(rw, C_FWO + lane * 1024 + i * 8, u32x2{(unsigned)tag, (unsigned)(tag >> 32)});
  }
  __builtin_amdgcn_s_setprio(2);
  {
    const int half = g8 >> 2, ct = g8 & 3;
    stage(0); stage(1); stage(2);
    const int kq = a & 3, T = ((a >> 2) & 1) * 8 + (a >> 5);
    const u32x4 srdA0 = mk_srd((kq >> 1) ? Wsim : Wsre, 8388608u);
    const unsigned row0 = (T * 128 + 32 * w) * 4096 + (kq & 1) * 2048;
    auto wsp = [&](int j, int i) {
      glds<false>(srdA0, (8 * i + rl) * 4096 + ((i & 1) ? sw1 : sw0), row0 + ((j ^ KXV) * 128), lb + j * 16384 + w * 4096 + i * 1024);
    };
    const int soff = (lane & 15) * 128 + (((lane >> 4) ^ ((lane & 15) >> 1)) << 4);
    f32x16 c0 = {0}, c1 = {0};
    f32x4 cs = {0};
    auto rda = [&](AFr& f, int slot, int ks) {
      const char* sb = shm + slot * 26624;
      f.w[ks] = *(const u32x4*)(sb + 8192 + w * 4096 + (xoff ^ (ks << 5)));
      f.x0[ks] = *(const u32x4*)(sb + (xoff ^ (ks << 5)));
      f.x1[ks] = *(const u32x4*)(sb + (xoff ^ (ks << 5)) + 4096);
      if (ks & 1) {
        f.sw[ks >> 1] = *(const u32x4*)(sb + 24576 + (soff ^ ((ks >> 1) << 6)));
        f.sx[ks >> 1] = *(const u32x4*)(sb + w * 2048 + (soff ^ ((ks >> 1) << 6)));
      }
    };
    int slot = 0;
    auto ita = [&](int k, AFr& cur, AFr& nxt, bool ld) {
      BARRIER();
      const int ns = slot == 2 ? 0 : slot + 1;
#pragma unroll
      for (int ks = 0; ks < 4; ++ks) {
        SB(); c0 = mfma32(cur.x0[ks], cur.w[ks], c0);
        SB(); rda(nxt, ns, ks);
        SB(); c1 = mfma32(cur.x1[ks], cur.w[ks], c1);
        SB();
        if (ld) { if (ks == 0) piece(k + 3, slot, 6); piece(k + 3, slot, ks + (ks >> 1)); }
        if (k == 6) wsp(0, ks);
        if (k == 7) { wsp(1, ks); wsp(2, ks); }
        if (ks & 1) {
          SB(); cs = mfma16(cur.sx[ks >> 1], cur.sw[ks >> 1], cs);
          SB();
          if (ld) piece(k + 3, slot, ks + (ks >> 1) + 1);
        }
      }
      SB();
      slot = ns;
    };
    AFr fa, fb;
    WAITV(12);
    BARRIER();
#pragma unroll
    for (int ks = 0; ks < 4; ++ks) rda(fa, 0, ks);
#pragma unroll
    for (int k = 0; k < 8; k += 2) {
      WAITV(6);
      ita(k, fa, fb, k < 6);
      if (k < 6) WAITV(6); else WAITV(4);
      ita(k + 1, fb, fa, k < 5);
    }
#pragma unroll
    for (int ks = 0; ks < 4; ++ks) {
      c0 = mfma32(fa.x0[ks], fa.w[ks], c0); c1 = mfma32(fa.x1[ks], fa.w[ks], c1);
      if (ks & 1) cs = mfma16(fa.sx[ks >> 1], fa.sw[ks >> 1], cs);
    }
    __builtin_amdgcn_s_setprio(2);
    const float sg = half ? -1.0f : 1.0f;
#pragma unroll
    for (int mi = 0; mi < 2; ++mi)
#pragma unroll
      for (int u = 0; u < 2; ++u)
        st16(rw, WS_ABB + ((half * 16 + 4 * ct + w) * 128 + R * 4 + mi * 2 + (r >> 4)) * 1024 + ((2 * u + h) * 16 + (r & 15)) * 16, pack_half(mi ? c1 : c0, u, sg));
    st8(rw, WS_ABS + (g8 * 64 + 2 * R + (w >> 1)) * 1024 + ((2 * (w & 1) + h) * 16 + (lane & 15)) * 16 + ((lane >> 4) & 1) * 8,
        u32x2{pk2(cs[0] * sg, cs[1] * sg), pk2(cs[2] * sg, cs[3] * sg)});
    WAITV(0);
    BARRIER();
    if (threadIdx.x == 0) set_flag(rw, C_FAB + (g8 * 32 + R) * 128, tag);
  }
  {
    const int kq = a & 3, T = ((a >> 2) & 1) * 8 + (a >> 5), ct = (a >> 3) & 3, half = kq >> 1, kh = kq & 1;
    u32x4 srdA = mk_srd(half ? Wsim : Wsre, 8388608u);
    const u32x4 srdB = mk_srd(ws + WS_ABB + ((half * 16 + 4 * ct + w) * 128 + kh * 64) * 1024, 65536u);
    const u32x4 srdS = mk_srd(ws + WS_ABS + ((half * 4 + ct) * 64 + kh * 32) * 1024, 32768u);
    const unsigned row0 = (T * 128 + 32 * w) * 4096 + kh * 2048;
    u32x4 voa;
#pragma unroll
    for (int i = 0; i < 4; ++i) voa[i] = (8 * i + (lane >> 3)) * 4096 + (((lane & 7) ^ (4 * (i & 1) + (lane >> 4))) << 4);
#pragma unroll
    for (int i = 0; i < 4; ++i) voa[i] -= i * 1024;
    const unsigned pb = WS_P + (T * 4 + ct) * 131072;
    if (w == 0) wait_flags(rw, C_FAB + ((half * 4 + ct) * 32 + kh * 16 + (lane & 15)) * 128, tag);
    WAITV(0);
    BARRIER();
    u32x4 soff = {row0 + 384, 0u, (unsigned)(w * 1024), row0 + 2048};
    u32x4 m0b = {lb + 49152 + w * 4096, lb + 65536 + w * 1024, (unsigned)(KXV << 7), (unsigned)(KXV << 12)};
    unsigned ab = lb + 32768 + (32 * w + (lane & 15)) * 128 + (((lane >> 4) ^ ((lane & 15) >> 1)) << 4);
    unsigned abs_ = lb + 65536 + 4096 + vl;
    f32x16 c[4]; f32x4 cs0, cs1;
    asm volatile(
    "s_nop 4\n"
    "v_mov_b32 v155, 0x7ffffff0\n"
    "s_xor_b32 s64, s53, s59\n"
    "s_mov_b32 m0, s57\n"
    "s_xor_b32 s57, s57, 0x1000\n"
    "s_lshl_b32 s63, s58, 4\n"
    "s_xor_b32 s63, s63, s54\n"
    "buffer_load_dwordx4 v149, s[44:47], s63 offen sc1 lds\n"
    "s_add_u32 s54, s54, 0x1000\n"
    "buffer_load_dword v154, v155, s[44:47], 0 offen\n"
    "buffer_load_dword v154, v155, s[44:47], 0 offen\n"
    "buffer_load_dword v154, v155, s[44:47], 0 offen\n"
    "buffer_load_dword v154, v155, s[44:47], 0 offen\n"
    "buffer_load_dword v154, v155, s[44:47], 0 offen\n"
    "buffer_load_dwordx4 v[72:75], v149, s[40:43], s64 offen offset:0 sc1\n"
    "buffer_load_dwordx4 v[76:79], v149, s[40:43], s64 offen offset:1024 sc1\n"
    "buffer_load_dword v154, v155, s[44:47], 0 offen\n"
    "buffer_load_dword v154, v155, s[44:47], 0 offen\n"
    "buffer_load_dwordx4 v[80:83], v149, s[40:43], s64 offen offset:2048 sc1\n"
    "buffer_load_dwordx4 v[84:87], v149, s[40:43], s64 offen offset:3072 sc1\n"
    "s_add_u32 s53, s53, 0x1000\n"
    "s_xor_b32 s64, s53, s59\n"
    "buffer_load_dword v154, v155, s[44:47], 0 offen\n"
    "buffer_load_dword v154, v155, s[44:47], 0 offen\n"
    "buffer_load_dwordx4 v[88:91], v149, s[40:43], s64 offen offset:0 sc1\n"
    "buffer_load_dwordx4 v[92:95], v149, s[40:43], s64 offen offset:1024 sc1\n"
    "buffer_load_dword v154, v155, s[44:47], 0 offen\n"
    "v_xor_b32 v141, 64, v140\n"
    "v_xor_b32 v142, 4096, v140\n"
    "v_xor_b32 v143, 4160, v140\n"
    "v_xor_b32 v144, 8192, v140\n"
    "v_xor_b32 v145, 8256, v140\n"
    "v_xor_b32 v146, 12288, v140\n"
    "v_xor_b32 v147, 12352, v140\n"
    "v_mov_b32 v0, 0\n"
    "v_mov_b32 v1, 0\n"
    "v_mov_b32 v2, 0\n"
    "v_mov_b32 v3, 0\n"
    "v_mov_b32 v4, 0\n"
    "v_mov_b32 v5, 0\n"
    "v_mov_b32 v6, 0\n"
    "v_mov_b32 v7, 0\n"
    "v_mov_b32 v8, 0\n"
    "v_mov_b32 v9, 0\n"
    "v_mov_b32 v10, 0\n"
    "v_mov_b32 v11, 0\n"
    "v_mov_b32 v12, 0\n"
    "v_mov_b32 v13, 0\n"
    "v_mov_b32 v14, 0\n"
    "v_mov_b32 v15, 0\n"
    "v_mov_b32 v16, 0\n"
    "v_mov_b32 v17, 0\n"
    "v_mov_b32 v18, 0\n"
    "v_mov_b32 v19, 0\n"
    "v_mov_b32 v20, 0\n"
    "v_mov_b32 v21, 0\n"
    "v_mov_b32 v22, 0\n"
    "v_mov_b32 v23, 0\n"
    "v_mov_b32 v24, 0\n"
    "v_mov_b32 v25, 0\n"
    "v_mov_b32 v26, 0\n"
    "v_mov_b32 v27, 0\n"
    "v_mov_b32 v28, 0\n"
    "v_mov_b32 v29, 0\n"
    "v_mov_b32 v30, 0\n"
    "v_mov_b32 v31, 0\n"
    "v_mov_b32 v32, 0\n"
    "v_mov_b32 v33, 0\n"
    "v_mov_b32 v34, 0\n"
    "v_mov_b32 v35, 0\n"
    "v_mov_b32 v36, 0\n"
    "v_mov_b32 v37, 0\n"
    "v_mov_b32 v38, 0\n"
    "v_mov_b32 v39, 0\n"
    "v_mov_b32 v40, 0\n"
    "v_mov_b32 v41, 0\n"
    "v_mov_b32 v42, 0\n"
    "v_mov_b32 v43, 0\n"
    "v_mov_b32 v44, 0\n"
    "v_mov_b32 v45, 0\n"
    "v_mov_b32 v46, 0\n"
    "v_mov_b32 v47, 0\n"
    "v_mov_b32 v48, 0\n"
    "v_mov_b32 v49, 0\n"
    "v_mov_b32 v50, 0\n"
    "v_mov_b32 v51, 0\n"
    "v_mov_b32 v52, 0\n"
    "v_mov_b32 v53, 0\n"
    "v_mov_b32 v54, 0\n"
    "v_mov_b32 v55, 0\n"
    "v_mov_b32 v56, 0\n"
    "v_mov_b32 v57, 0\n"
    "v_mov_b32 v58, 0\n"
    "v_mov_b32 v59, 0\n"
    "v_mov_b32 v60, 0\n"
    "v_mov_b32 v61, 0\n"
    "v_mov_b32 v62, 0\n"
    "v_mov_b32 v63, 0\n"
    "v_mov_b32 v64, 0\n"
    "v_mov_b32 v65, 0\n"
    "v_mov_b32 v66, 0\n"
    "v_mov_b32 v67, 0\n"
    "v_mov_b32 v68, 0\n"
    "v_mov_b32 v69, 0\n"
    "v_mov_b32 v70, 0\n"
    "v_mov_b32 v71, 0\n"
    "v_mov_b32 v96, 0\n"
    "v_mov_b32 v97, 0\n"
    "v_mov_b32 v98, 0\n"
    "v_mov_b32 v99, 0\n"
    "v_mov_b32 v100, 0\n"
    "v_mov_b32 v101, 0\n"
    "v_mov_b32 v102, 0\n"
    "v_mov_b32 v103, 0\n"
    "v_mov_b32 v120, 0\n"
    "v_mov_b32 v121, 0\n"
    "v_mov_b32 v122, 0\n"
    "v_mov_b32 v123, 0\n"
    "v_mov_b32 v124, 0\n"
    "v_mov_b32 v125, 0\n"
    "v_mov_b32 v126, 0\n"
    "v_mov_b32 v127, 0\n"
    "v_mov_b32 v128, 0\n"
    "v_mov_b32 v129, 0\n"
    "v_mov_b32 v130, 0\n"
    "v_mov_b32 v131, 0\n"
    "v_mov_b32 v132, 0\n"
    "v_mov_b32 v133, 0\n"
    "v_mov_b32 v134, 0\n"
    "v_mov_b32 v135, 0\n"
    "s_mov_b32 s60, 8\n"
    "LGEMM_%=:\n"
    "s_waitcnt vmcnt(16) lgkmcnt(0)\n"
    "s_barrier\n"
    "v_mfma_f32_16x16x32_bf16 v[32:35], v[96:99], v[120:123], v[32:35]\n"
    "v_mfma_f32_16x16x32_bf16 v[36:39], v[100:103], v[120:123], v[36:39]\n"
    "s_setprio 2\n"
    "v_xor_b32 v140, 0x8000, v140\n"
    "v_xor_b32 v141, 0x8000, v141\n"
    "v_xor_b32 v142, 0x8000, v142\n"
    "v_xor_b32 v143, 0x8000, v143\n"
    "v_xor_b32 v144, 0x8000, v144\n"
    "v_xor_b32 v145, 0x8000, v145\n"
    "v_xor_b32 v146, 0x8000, v146\n"
    "v_xor_b32 v147, 0x8000, v147\n"
    "v_xor_b32 v148, 0x1000, v148\n"
    "s_setprio 2\n"
    "ds_read_b128 v[104:107], v140 offset:0\n"
    "ds_read_b128 v[108:111], v140 offset:2048\n"
    "ds_read_b128 v[112:115], v142 offset:0\n"
    "ds_read_b128 v[116:119], v142 offset:2048\n"
    "ds_read_b128 v[136:139], v148 offset:0\n"
    "v_mfma_f32_16x16x32_bf16 v[40:43], v[96:99], v[124:127], v[40:43]\n"
    "v_mfma_f32_16x16x32_bf16 v[44:47], v[100:103], v[124:127], v[44:47]\n"
    "s_mov_b32 m0, s57\n"
    "s_xor_b32 s57, s57, 0x1000\n"
    "s_lshl_b32 s63, s58, 4\n"
    "s_xor_b32 s63, s63, s54\n"
    "buffer_load_dwordx4 v149, s[44:47], s63 offen sc1 lds\n"
    "s_add_u32 s54, s54, 0x1000\n"
    "s_cmp_ge_u32 s52, s55\n"
    "s_cselect_b32 s38, 0, s38\n"
    "s_mov_b32 m0, s56\n"
    "s_xor_b32 s62, s52, s58\n"
    "buffer_load_dwordx4 v150, s[36:39], s62 offen offset:0 lds\n"
    "v_mfma_f32_16x16x32_bf16 v[48:51], v[96:99], v[128:131], v[48:51]\n"
    "v_mfma_f32_16x16x32_bf16 v[56:59], v[96:99], v[132:135], v[56:59]\n"
    "buffer_load_dwordx4 v[96:99], v149, s[40:43], s64 offen offset:2048 sc1\n"
    "v_mfma_f32_16x16x32_bf16 v[52:55], v[100:103], v[128:131], v[52:55]\n"
    "v_mfma_f32_16x16x32_bf16 v[60:63], v[100:103], v[132:135], v[60:63]\n"
    "buffer_load_dwordx4 v[100:103], v149, s[40:43], s64 offen offset:3072 sc1\n"
    "s_add_u32 s53, s53, 0x1000\n"
    "s_xor_b32 s64, s53, s59\n"
    "s_waitcnt vmcnt(13) lgkmcnt(0)\n"
    "v_mfma_f32_16x16x32_bf16 v[0:3], v[72:75], v[104:107], v[0:3]\n"
    "v_mfma_f32_16x16x32_bf16 v[4:7], v[76:79], v[104:107], v[4:7]\n"
    "ds_read_b128 v[120:123], v144 offset:0\n"
    "ds_read_b128 v[124:127], v144 offset:2048\n"
    "ds_read_b128 v[128:131], v146 offset:0\n"
    "ds_read_b128 v[132:135], v146 offset:2048\n"
    "v_mfma_f32_16x16x32_bf16 v[8:11], v[72:75], v[108:111], v[8:11]\n"
    "v_mfma_f32_16x16x32_bf16 v[12:15], v[76:79], v[108:111], v[12:15]\n"
    "v_mfma_f32_16x16x32_bf16 v[64:67], v[136:139], v[104:107], v[64:67]\n"
    "v_mfma_f32_16x16x32_bf16 v[68:71], v[136:139], v[108:111], v[68:71]\n"
    "buffer_load_dwordx4 v151, s[36:39], s62 offen offset:1024 lds\n"
    "v_mfma_f32_16x16x32_bf16 v[16:19], v[72:75], v[112:115], v[16:19]\n"
    "v_mfma_f32_16x16x32_bf16 v[24:27], v[72:75], v[116:119], v[24:27]\n"
    "v_mfma_f32_16x16x32_bf16 v[20:23], v[76:79], v[112:115], v[20:23]\n"
    "v_mfma_f32_16x16x32_bf16 v[28:31], v[76:79], v[116:119], v[28:31]\n"
    "s_waitcnt lgkmcnt(0)\n"
    "v_mfma_f32_16x16x32_bf16 v[32:35], v[72:75], v[120:123], v[32:35]\n"
    "v_mfma_f32_16x16x32_bf16 v[36:39], v[76:79], v[120:123], v[36:39]\n"
    "ds_read_b128 v[104:107], v141 offset:0\n"
    "ds_read_b128 v[108:111], v141 offset:2048\n"
    "ds_read_b128 v[112:115], v143 offset:0\n"
    "ds_read_b128 v[116:119], v143 offset:2048\n"
    "ds_read_b128 v[136:139], v148 offset:1024\n"
    "v_mfma_f32_16x16x32_bf16 v[40:43], v[72:75], v[124:127], v[40:43]\n"
    "v_mfma_f32_16x16x32_bf16 v[44:47], v[76:79], v[124:127], v[44:47]\n"
    "buffer_load_dwordx4 v152, s[36:39], s62 offen offset:2048 lds\n"
    "v_mfma_f32_16x16x32_bf16 v[48:51], v[72:75], v[128:131], v[48:51]\n"
    "v_mfma_f32_16x16x32_bf16 v[56:59], v[72:75], v[132:135], v[56:59]\n"
    "buffer_load_dwordx4 v[72:75], v149, s[40:43], s64 offen offset:0 sc1\n"
    "v_mfma_f32_16x16x32_bf16 v[52:55], v[76:79], v[128:131], v[52:55]\n"
    "v_mfma_f32_16x16x32_bf16 v[60:63], v[76:79], v[132:135], v[60:63]\n"
    "buffer_load_dwordx4 v[76:79], v149, s[40:43], s64 offen offset:1024 sc1\n"
    "s_waitcnt vmcnt(13) lgkmcnt(0)\n"
    "v_mfma_f32_16x16x32_bf16 v[0:3], v[80:83], v[104:107], v[0:3]\n"
    "v_mfma_f32_16x16x32_bf16 v[4:7], v[84:87], v[104:107], v[4:7]\n"
    "ds_read_b128 v[120:123], v145 offset:0\n"
    "ds_read_b128 v[124:127], v145 offset:2048\n"
    "ds_read_b128 v[128:131], v147 offset:0\n"
    "ds_read_b128 v[132:135], v147 offset:2048\n"
    "v_mfma_f32_16x16x32_bf16 v[8:11], v[80:83], v[108:111], v[8:11]\n"
    "v_mfma_f32_16x16x32_bf16 v[12:15], v[84:87], v[108:111], v[12:15]\n"
    "v_mfma_f32_16x16x32_bf16 v[64:67], v[136:139], v[104:107], v[64:67]\n"
    "v_mfma_f32_16x16x32_bf16 v[68:71], v[136:139], v[108:111], v[68:71]\n"
    "buffer_load_dwordx4 v153, s[36:39], s62 offen offset:3072 lds\n"
    "s_add_u32 s52, s52, 128\n"
    "s_add_u32 s56, s56, 0x4000\n"
    "s_and_b32 s56, s56, 0xffff\n"
    "v_mfma_f32_16x16x32_bf16 v[16:19], v[80:83], v[112:115], v[16:19]\n"
    "v_mfma_f32_16x16x32_bf16 v[24:27], v[80:83], v[116:119], v[24:27]\n"
    "v_mfma_f32_16x16x32_bf16 v[20:23], v[84:87], v[112:115], v[20:23]\n"
    "v_mfma_f32_16x16x32_bf16 v[28:31], v[84:87], v[116:119], v[28:31]\n"
    "s_waitcnt vmcnt(17) lgkmcnt(0)\n"
    "s_barrier\n"
    "v_mfma_f32_16x16x32_bf16 v[32:35], v[80:83], v[120:123], v[32:35]\n"
    "v_mfma_f32_16x16x32_bf16 v[36:39], v[84:87], v[120:123], v[36:39]\n"
    "ds_read_b128 v[104:107], v140 offset:16384\n"
    "ds_read_b128 v[108:111], v140 offset:18432\n"
    "ds_read_b128 v[112:115], v142 offset:16384\n"
    "ds_read_b128 v[116:119], v142 offset:18432\n"
    "ds_read_b128 v[136:139], v148 offset:2048\n"
    "v_mfma_f32_16x16x32_bf16 v[40:43], v[80:83], v[124:127], v[40:43]\n"
    "v_mfma_f32_16x16x32_bf16 v[44:47], v[84:87], v[124:127], v[44:47]\n"
    "s_cmp_ge_u32 s52, s55\n"
    "s_cselect_b32 s38, 0, s38\n"
    "s_mov_b32 m0, s56\n"
    "s_xor_b32 s62, s52, s58\n"
    "buffer_load_dwordx4 v150, s[36:39], s62 offen offset:0 lds\n"
    "v_mfma_f32_16x16x32_bf16 v[48:51], v[80:83], v[128:131], v[48:51]\n"
    "v_mfma_f32_16x16x32_bf16 v[56:59], v[80:83], v[132:135], v[56:59]\n"
    "buffer_load_dwordx4 v[80:83], v149, s[40:43], s64 offen offset:2048 sc1\n"
    "v_mfma_f32_16x16x32_bf16 v[52:55], v[84:87], v[128:131], v[52:55]\n"
    "v_mfma_f32_16x16x32_bf16 v[60:63], v[84:87], v[132:135], v[60:63]\n"
    "buffer_load_dwordx4 v[84:87], v149, s[40:43], s64 offen offset:3072 sc1\n"
    "s_add_u32 s53, s53, 0x1000\n"
    "s_xor_b32 s64, s53, s59\n"
    "s_setprio 0\n"
    "s_waitcnt vmcnt(13) lgkmcnt(0)\n"
    "v_mfma_f32_16x16x32_bf16 v[0:3], v[88:91], v[104:107], v[0:3]\n"
    "v_mfma_f32_16x16x32_bf16 v[4:7], v[92:95], v[104:107], v[4:7]\n"
    "ds_read_b128 v[120:123], v144 offset:16384\n"
    "ds_read_b128 v[124:127], v144 offset:18432\n"
    "ds_read_b128 v[128:131], v146 offset:16384\n"
    "ds_read_b128 v[132:135], v146 offset:18432\n"
    "v_mfma_f32_16x16x32_bf16 v[8:11], v[88:91], v[108:111], v[8:11]\n"
    "v_mfma_f32_16x16x32_bf16 v[12:15], v[92:95], v[108:111], v[12:15]\n"
    "v_mfma_f32_16x16x32_bf16 v[64:67], v[136:139], v[104:107], v[64:67]\n"
    "v_mfma_f32_16x16x32_bf16 v[68:71], v[136:139], v[108:111], v[68:71]\n"
    "buffer_load_dwordx4 v151, s[36:39], s62 offen offset:1024 lds\n"
    "v_mfma_f32_16x16x32_bf16 v[16:19], v[88:91], v[112:115], v[16:19]\n"
    "v_mfma_f32_16x16x32_bf16 v[24:27], v[88:91], v[116:119], v[24:27]\n"
    "v_mfma_f32_16x16x32_bf16 v[20:23], v[92:95], v[112:115], v[20:23]\n"
    "v_mfma_f32_16x16x32_bf16 v[28:31], v[92:95], v[116:119], v[28:31]\n"
    "s_waitcnt lgkmcnt(0)\n"
    "v_mfma_f32_16x16x32_bf16 v[32:35], v[88:91], v[120:123], v[32:35]\n"
    "v_mfma_f32_16x16x32_bf16 v[36:39], v[92:95], v[120:123], v[36:39]\n"
    "ds_read_b128 v[104:107], v141 offset:16384\n"
    "ds_read_b128 v[108:111], v141 offset:18432\n"
    "ds_read_b128 v[112:115], v143 offset:16384\n"
    "ds_read_b128 v[116:119], v143 offset:18432\n"
    "ds_read_b128 v[136:139], v148 offset:3072\n"
    "v_mfma_f32_16x16x32_bf16 v[40:43], v[88:91], v[124:127], v[40:43]\n"
    "v_mfma_f32_16x16x32_bf16 v[44:47], v[92:95], v[124:127], v[44:47]\n"
    "buffer_load_dwordx4 v152, s[36:39], s62 offen offset:2048 lds\n"
    "v_mfma_f32_16x16x32_bf16 v[48:51], v[88:91], v[128:131], v[48:51]\n"
    "v_mfma_f32_16x16x32_bf16 v[56:59], v[88:91], v[132:135], v[56:59]\n"
    "buffer_load_dwordx4 v[88:91], v149, s[40:43], s64 offen offset:0 sc1\n"
    "v_mfma_f32_16x16x32_bf16 v[52:55], v[92:95], v[128:131], v[52:55]\n"
    "v_mfma_f32_16x16x32_bf16 v[60:63], v[92:95], v[132:135], v[60:63]\n"
    "buffer_load_dwordx4 v[92:95], v149, s[40:43], s64 offen offset:1024 sc1\n"
    "s_waitcnt vmcnt(12) lgkmcnt(0)\n"
    "v_mfma_f32_16x16x32_bf16 v[0:3], v[96:99], v[104:107], v[0:3]\n"
    "v_mfma_f32_16x16x32_bf16 v[4:7], v[100:103], v[104:107], v[4:7]\n"
    "ds_read_b128 v[120:123], v145 offset:16384\n"
    "ds_read_b128 v[124:127], v145 offset:18432\n"
    "ds_read_b128 v[128:131], v147 offset:16384\n"
    "ds_read_b128 v[132:135], v147 offset:18432\n"
    "v_mfma_f32_16x16x32_bf16 v[8:11], v[96:99], v[108:111], v[8:11]\n"
    "v_mfma_f32_16x16x32_bf16 v[12:15], v[100:103], v[108:111], v[12:15]\n"
    "v_mfma_f32_16x16x32_bf16 v[64:67], v[136:139], v[104:107], v[64:67]\n"
    "v_mfma_f32_16x16x32_bf16 v[68:71], v[136:139], v[108:111], v[68:71]\n"
    "buffer_load_dwordx4 v153, s[36:39], s62 offen offset:3072 lds\n"
    "s_add_u32 s52, s52, 128\n"
    "s_add_u32 s56, s56, 0x4000\n"
    "s_and_b32 s56, s56, 0xffff\n"
    "v_mfma_f32_16x16x32_bf16 v[16:19], v[96:99], v[112:115], v[16:19]\n"
    "v_mfma_f32_16x16x32_bf16 v[24:27], v[96:99], v[116:119], v[24:27]\n"
    "v_mfma_f32_16x16x32_bf16 v[20:23], v[100:103], v[112:115], v[20:23]\n"
    "v_mfma_f32_16x16x32_bf16 v[28:31], v[100:103], v[116:119], v[28:31]\n"
    "s_sub_u32 s60, s60, 1\n"
    "s_cmp_lg_u32 s60, 0\n"
    "s_cbranch_scc1 LGEMM_%=\n"
    "s_setprio 2\n"
    "s_waitcnt vmcnt(0) lgkmcnt(0)\n"
    "v_mfma_f32_16x16x32_bf16 v[32:35], v[96:99], v[120:123], v[32:35]\n"
    "v_mfma_f32_16x16x32_bf16 v[36:39], v[100:103], v[120:123], v[36:39]\n"
    "v_mfma_f32_16x16x32_bf16 v[40:43], v[96:99], v[124:127], v[40:43]\n"
    "v_mfma_f32_16x16x32_bf16 v[44:47], v[100:103], v[124:127], v[44:47]\n"
    "v_mfma_f32_16x16x32_bf16 v[48:51], v[96:99], v[128:131], v[48:51]\n"
    "v_mfma_f32_16x16x32_bf16 v[52:55], v[100:103], v[128:131], v[52:55]\n"
    "v_mfma_f32_16x16x32_bf16 v[56:59], v[96:99], v[132:135], v[56:59]\n"
    "v_mfma_f32_16x16x32_bf16 v[60:63], v[100:103], v[132:135], v[60:63]\n"
    "s_nop 15\n"
    "s_nop 3\n"
        : "={v[0:15]}"(c[0]), "={v[16:31]}"(c[1]), "={v[32:47]}"(c[2]), "={v[48:63]}"(c[3]), "={v[64:67]}"(cs0), "={v[68:71]}"(cs1),
          "+{v140}"(ab), "+{v148}"(abs_), "+{s[36:39]}"(srdA), "+{s[52:55]}"(soff), "+{s[56:59]}"(m0b)
        : "{v[150:153]}"(voa), "{v149}"(vl), "{s[40:43]}"(srdB), "{s[44:47]}"(srdS)
        : "v72", "v73", "v74", "v75", "v76", "v77", "v78", "v79", "v80", "v81", "v82", "v83", "v84", "v85", "v86", "v87", "v88", "v89", "v90", "v91", "v92", "v93", "v94", "v95", "v96", "v97", "v98", "v99", "v100", "v101", "v102", "v103", "v104", "v105", "v106", "v107", "v108", "v109", "v110", "v111", "v112", "v113", "v114", "v115", "v116", "v117", "v118", "v119", "v120", "v121", "v122", "v123", "v124", "v125", "v126", "v127", "v128", "v129", "v130", "v131", "v132", "v133", "v134", "v135", "v136", "v137", "v138", "v139", "v141", "v142", "v143", "v144", "v145", "v146", "v147", "v154", "v155", "s60", "s62", "s63", "s64", "scc", "memory");
    f32x16 own = c[0];
#pragma unroll
    for (int rg = 0; rg < 4; ++rg) {
      if ((rg ^ w) == kq) own = c[rg];
      else {
        const f32x16& x = c[rg];
        unsigned o = pb + (kq * 4 + w) * 8192 + (((rg ^ w) - kq - 1) & 3) * 2048 + vl;
        st16(rw, o, u32x4{pk2(x[0], x[1]), pk2(x[2], x[3]), pk2(x[4], x[5]), pk2(x[6], x[7])});
        st16(rw, o + 1024, u32x4{pk2(x[8], x[9]), pk2(x[10], x[11]), pk2(x[12], x[13]), pk2(x[14], x[15])});
      }
    }
    if (w != kq) st16(rw, pb + (kq * 4 + w) * 8192 + 6144 + vl, u32x4{pk2(cs0[0], cs0[1]), pk2(cs0[2], cs0[3]), pk2(cs1[0], cs1[1]), pk2(cs1[2], cs1[3])});
    WAITV(0);
    BARRIER();
    if (threadIdx.x == 0) set_flag(rw, C_FP + ((T * 4 + ct) * 4 + kq) * 128, tag);
    if (w == 0) wait_flags(rw, C_FP + ((T * 4 + ct) * 4 + (lane & 3)) * 128, tag);
    BARRIER();
    u32x4 pp[3][2], ps[3];
#pragma unroll
    for (int s = 0; s < 3; ++s) {
      unsigned o = pb + ((kq + 1 + s) & 3) * 32768 + vl;
      pp[s][0] = __builtin_amdgcn_raw_buffer_load_b128(rw, o + w * 8192 + (2 - s) * 2048, 0, 16);
      pp[s][1] = __builtin_amdgcn_raw_buffer_load_b128(rw, o + w * 8192 + (2 - s) * 2048 + 1024, 0, 16);
      ps[s] = __builtin_amdgcn_raw_buffer_load_b128(rw, o + kq * 8192 + 6144, 0, 16);
    }
#pragma unroll
    for (int s = 0; s < 3; ++s) {
#pragma unroll
      for (int i = 0; i < 8; ++i) { own[2 * i] += bf_lo(pp[s][i >> 2][i & 3]); own[2 * i + 1] += bf_hi(pp[s][i >> 2][i & 3]); }
      cs0[0] += bf_lo(ps[s][0]); cs0[1] += bf_hi(ps[s][0]); cs0[2] += bf_lo(ps[s][1]); cs0[3] += bf_hi(ps[s][1]);
      cs1[0] += bf_lo(ps[s][2]); cs1[1] += bf_hi(ps[s][2]); cs1[2] += bf_lo(ps[s][3]); cs1[3] += bf_hi(ps[s][3]);
    }
    const unsigned o = (T * 128 + 32 * kq) * 2304 + 1152;
#pragma unroll
    for (int cg = 0; cg < 2; ++cg) {
      auto s0 = __builtin_amdgcn_permlane16_swap(pk2(own[4 * cg], own[4 * cg + 1]), pk2(own[8 + 4 * cg], own[8 + 4 * cg + 1]), false, false);
      auto s1 = __builtin_amdgcn_permlane16_swap(pk2(own[4 * cg + 2], own[4 * cg + 3]), pk2(own[8 + 4 * cg + 2], own[8 + 4 * cg + 3]), false, false);
      __builtin_amdgcn_raw_buffer_store_b128(u32x4{s0[0], s1[0], s0[1], s1[1]}, ro, o + r * 2304 + ((4 * ct + w) * 32 + 16 * cg + 8 * h) * 2, 0, 0);
    }
    if (w == kq) {
      const unsigned os = o + (lane & 15) * 2304 + (512 + 16 * ct + 4 * (lane >> 4)) * 2;
      __builtin_amdgcn_raw_buffer_store_b64(u32x2{pk2(cs0[0], cs0[1]), pk2(cs0[2], cs0[3])}, ro, os, 0, 0);
      __builtin_amdgcn_raw_buffer_store_b64(u32x2{pk2(cs1[0], cs1[1]), pk2(cs1[2], cs1[3])}, ro, os + 16 * 2304, 0, 0);
    }
  }
}

extern "C" void kernel_launch(void* const* d_in, const int* in_sizes, int n_in, void* d_out, int out_size, void* d_ws,
                              size_t ws_size, hipStream_t stream) {
  (void)in_sizes; (void)n_in; (void)out_size; (void)ws_size;
  fused_kernel<<<512, 256, 0, stream>>>((const char*)d_in[0], (const char*)d_in[1], (const char*)d_in[2], (const char*)d_in[3],
                                        (const char*)d_in[4], (const char*)d_in[5], (const char*)d_in[6], (const char*)d_in[7],
                                        (const char*)d_in[8], (const char*)d_in[9], (char*)d_out, (char*)d_ws + 0);
}
```
